```python
import torch

def _softmax(x, axis=-1):
    return torch.softmax(x, dim=axis)
def _gelu(x):
    return torch.nn.functional.gelu(x, approximate="tanh")
def _silu(x):
    return torch.nn.functional.silu(x)
def _sigmoid(x):
    return torch.sigmoid(x)
def _layernorm(x, axis=-1, eps=1e-5):
    m = x.mean(dim=axis, keepdim=True)
    v = x.var(dim=axis, keepdim=True, unbiased=False)
    return (x - m) / torch.sqrt(v + eps)
def _scatter_add(tgt, idx, src, axis=0):
    return tgt.clone().index_add_(axis, idx, src)
def _segment_sum(src, seg, n):
    out = torch.zeros((n,) + src.shape[1:], dtype=src.dtype)
    return out.index_add_(0, seg, src)
def _pad(x, pw):
    flat = tuple(v for pair in reversed(pw) for v in pair)
    return torch.nn.functional.pad(x, flat)

kernel_name = 'sequential_preLN__ssm_conv_h3__glu_gated_swiglu__prompt'

def setup_inputs():

    _w = (lambda a: (lambda t: t.to(torch.bfloat16) if t.is_floating_point() else t)(torch.as_tensor(a)))
    def _A_setup():
        B, S, D = 1, 2048, 448
        ks = 4
        return {
            "q": _w((lambda *s: torch.randn(*s, dtype=torch.bfloat16) * 0.05)(B, S, D)),
            "k": _w((lambda *s: torch.randn(*s, dtype=torch.bfloat16) * 0.2)(B, S, D)),
            "v": _w((lambda *s: torch.randn(*s, dtype=torch.bfloat16) * 0.2)(B, S, D)),
            "shift_k": _w((lambda *s: torch.randn(*s, dtype=torch.bfloat16) * 0.2)(D, ks) * 0.5),
            "ssm_k": _w((lambda *s: torch.randn(*s, dtype=torch.bfloat16) * 0.4)(D, S) * (1.0 / S ** 0.5)),
            "ssm_D": _w((lambda *s: torch.randn(*s, dtype=torch.bfloat16) * 0.2)(D)),
        }
    _A_in = _A_setup()

    def _B_setup():
        B, S, D, F = 1, 2048, 448, 1792
        return {
            "x": _w((lambda *s: torch.randn(*s, dtype=torch.bfloat16) * 0.05)(B, S, D)),
            "w1": _w((lambda *s: torch.randn(*s, dtype=torch.bfloat16) * 0.2)(D, F) * (2.0 / D) ** 0.5),
            "w3": _w((lambda *s: torch.randn(*s, dtype=torch.bfloat16) * 0.2)(D, F) * (2.0 / D) ** 0.5),
            "w2": _w((lambda *s: torch.randn(*s, dtype=torch.bfloat16) * 0.2)(F, D) * (2.0 / F) ** 0.5),
        }
    _B_in = _B_setup()

    return {
        "A_q": _A_in["q"],
        "A_k": _A_in["k"],
        "A_v": _A_in["v"],
        "A_shift_k": _A_in["shift_k"],
        "A_ssm_k": _A_in["ssm_k"],
        "A_ssm_D": _A_in["ssm_D"],
        "B_x": _B_in["x"],
        "B_w1": _B_in["w1"],
        "B_w3": _B_in["w3"],
        "B_w2": _B_in["w2"],
    }

def reference(A_q, A_k, A_v, A_shift_k, A_ssm_k, A_ssm_D, B_x, B_w1, B_w3, B_w2):
    def _A_ref(q, k, v, shift_k, ssm_k, ssm_D):
        B, S, D = q.shape
        ks = shift_k.shape[-1]
        k_pad = _pad(k, ((0, 0), (ks - 1, 0), (0, 0)))
        k_shift = torch.zeros_like(k)
        for t in range(ks):
            k_shift = k_shift + k_pad[:, t:t + S, :] * shift_k[:, ks - 1 - t][None, None, :]
        kv = k_shift * v
        L = 2 * S
        kv_pad = _pad(kv, ((0, 0), (0, S), (0, 0)))
        kf_pad = _pad(ssm_k, ((0, 0), (0, S)))
        kv_f = (lambda x, axes=(-2,-1): torch.fft.rfft2(x.float(), dim=axes))(kv_pad[:, None, :, :], axes=(1, 2))
        kf_f = (lambda x, axes=(-2,-1): torch.fft.rfft2(x.float(), dim=axes))(kf_pad[:, None, :], axes=(1, 2))
        kf_f = (lambda x, *d: x.permute(*d))(kf_f, 1, 2, 0)
        y_f = kv_f * kf_f[None, :, :, :]
        y = (lambda x, s, axes=(-2,-1): torch.fft.irfft2(x, s=s, dim=axes).to(torch.bfloat16))(y_f, (1, L), axes=(1, 2))[:, 0, :S, :]
        y = y + kv * ssm_D[None, None, :]
        return q * y

    def _B_ref(x, w1, w3, w2):
        gate = torch.einsum("bsd,df->bsf", x, w1)
        up = torch.einsum("bsd,df->bsf", x, w3)
        h = _silu(gate) * up
        out = torch.einsum("bsf,fd->bsd", h, w2)
        return out

    x = A_q
    h = x + _A_ref(_layernorm(x, axis=-1), A_k, A_v, A_shift_k, A_ssm_k, A_ssm_D)
    h2 = h + _B_ref(_layernorm(h, axis=-1), B_w1, B_w3, B_w2)
    return h2
```

```cpp
#include <hip/hip_runtime.h>
#include <hip/hip_bf16.h>
#include <stdint.h>

typedef unsigned short u16;
typedef unsigned int u32;
typedef u32 u32x2 __attribute__((ext_vector_type(2)));
typedef u32 u32x4 __attribute__((ext_vector_type(4)));
typedef float f32x4 __attribute__((ext_vector_type(4)));
typedef float f32x16 __attribute__((ext_vector_type(16)));
typedef __bf16 bf16x8 __attribute__((ext_vector_type(8)));
typedef __bf16 bf16x2 __attribute__((ext_vector_type(2)));
#define DI __device__ __forceinline__
#define LAS __attribute__((address_space(3)))

constexpr int S = 2048, D = 448, F = 1792;
constexpr int NKS1 = 28;
constexpr int NKA = 29;
constexpr int NFT = 112;
constexpr size_t SZ_SD = (size_t)S * D * 2;
constexpr size_t OFF_U = 0;
constexpr size_t OFF_X = OFF_U + SZ_SD;
constexpr size_t OFF_G = OFF_X + SZ_SD;
constexpr size_t OFF_H = OFF_G + SZ_SD;
constexpr size_t OFF_W13 = OFF_H + SZ_SD;
constexpr size_t OFF_W2 = OFF_W13 + (size_t)NFT * NKA * 1024;
constexpr size_t OFF_A = OFF_W2 + (size_t)14 * 112 * 1024;

DI float bf_lo(u32 v) { return __uint_as_float(v << 16); }
DI float bf_hi(u32 v) { return __uint_as_float(v & 0xffff0000u); }
DI u32 pk2(float lo, float hi) {
  bf16x2 r; r[0] = (__bf16)lo; r[1] = (__bf16)hi; return __builtin_bit_cast(u32, r);
}
DI float rbf(float x) { return (float)(__bf16)x; }
DI f32x16 mfma32(u32x4 a, u32x4 b, f32x16 c) {
  return __builtin_amdgcn_mfma_f32_32x32x16_bf16(__builtin_bit_cast(bf16x8, a), __builtin_bit_cast(bf16x8, b), c, 0, 0, 0);
}
DI f32x4 mfma16(u32x4 a, u32x4 b, f32x4 c) {
  return __builtin_amdgcn_mfma_f32_16x16x32_bf16(__builtin_bit_cast(bf16x8, a), __builtin_bit_cast(bf16x8, b), c, 0, 0, 0);
}

DI void prep_tile(int b, const u16* q, const u16* k, const u16* v, const u16* shk, char* ws, float* lds) {
  const int tid = threadIdx.x, lane = tid & 63, w = tid >> 6;
  const int xcd = b & 7, i = b >> 3;
  const int tb = xcd * 8 + (i >> 2), cq = i & 3;
  const int t0 = tb * 32;
  const int srow = tid >> 3, sj = tid & 7;
  u32x4 sq[7];
  {
    const u32x4* p = (const u32x4*)(q + (size_t)(t0 + srow) * D) + sj;
#pragma unroll
    for (int c = 0; c < 7; ++c) sq[c] = p[8 * c];
  }
  const int o = lane & 3, cp = (lane >> 2) + 16 * w;
  const bool act = cp < 56;
  const int ch = cq * 112 + 2 * (act ? cp : 0);
  const int r0 = t0 + 8 * o;
  u32 kk[11], vv[8], qq[8];
#pragma unroll
  for (int j = 0; j < 11; ++j) {
    int row = r0 - 3 + j;
    u32 val = *(const u32*)(k + (size_t)(row < 0 ? 0 : row) * D + ch);
    kk[j] = row < 0 ? 0u : val;
  }
#pragma unroll
  for (int j = 0; j < 8; ++j) { vv[j] = *(const u32*)(v + (size_t)(r0 + j) * D + ch); qq[j] = *(const u32*)(q + (size_t)(r0 + j) * D + ch); }
  const u32x4 sk = *(const u32x4*)(shk + ch * 4);
  float s = 0.f;
#pragma unroll
  for (int c = 0; c < 7; ++c)
#pragma unroll
    for (int e = 0; e < 4; ++e) s += bf_lo(sq[c][e]) + bf_hi(sq[c][e]);
  s += __shfl_xor(s, 1); s += __shfl_xor(s, 2); s += __shfl_xor(s, 4);
  const float mean = s * (1.0f / D);
  float ss = 0.f;
#pragma unroll
  for (int c = 0; c < 7; ++c)
#pragma unroll
    for (int e = 0; e < 4; ++e) { float a = bf_lo(sq[c][e]) - mean, bb = bf_hi(sq[c][e]) - mean; ss += a * a + bb * bb; }
  ss += __shfl_xor(ss, 1); ss += __shfl_xor(ss, 2); ss += __shfl_xor(ss, 4);
  if (sj == 0) {
    float var = rbf(ss * (1.0f / D));
    lds[2 * srow] = rbf(mean);
    lds[2 * srow + 1] = rbf(sqrtf(rbf(var + 1e-5f)));
  }
  __syncthreads();
  if (!act) return;
  float u0[8], u1[8], g0[8], g1[8];
  const float a0 = bf_lo(sk[0]), a1 = bf_hi(sk[0]), a2 = bf_lo(sk[1]), a3 = bf_hi(sk[1]);
  const float c0 = bf_lo(sk[2]), c1 = bf_hi(sk[2]), c2 = bf_lo(sk[3]), c3 = bf_hi(sk[3]);
#pragma unroll
  for (int j = 0; j < 8; ++j) {
    float x0 = rbf(bf_lo(kk[j]) * a3);
    x0 = rbf(x0 + rbf(bf_lo(kk[j + 1]) * a2));
    x0 = rbf(x0 + rbf(bf_lo(kk[j + 2]) * a1));
    x0 = rbf(x0 + rbf(bf_lo(kk[j + 3]) * a0));
    float x1 = rbf(bf_hi(kk[j]) * c3);
    x1 = rbf(x1 + rbf(bf_hi(kk[j + 1]) * c2));
    x1 = rbf(x1 + rbf(bf_hi(kk[j + 2]) * c1));
    x1 = rbf(x1 + rbf(bf_hi(kk[j + 3]) * c0));
    u0[j] = x0 * bf_lo(vv[j]); u1[j] = x1 * bf_hi(vv[j]);
    const float m = lds[2 * (8 * o + j)], sd = lds[2 * (8 * o + j) + 1];
    g0[j] = rbf(bf_lo(qq[j]) - m) / sd; g1[j] = rbf(bf_hi(qq[j]) - m) / sd;
  }
  const size_t off = ((size_t)(tb * D + ch) * 32 + 8 * o) * 2;
  u32x4 r;
#pragma unroll
  for (int e = 0; e < 4; ++e) r[e] = pk2(u0[7 - 2 * e], u0[6 - 2 * e]);
  *(u32x4*)(ws + OFF_U + off) = r;
#pragma unroll
  for (int e = 0; e < 4; ++e) r[e] = pk2(u1[7 - 2 * e], u1[6 - 2 * e]);
  *(u32x4*)(ws + OFF_U + off + 64) = r;
#pragma unroll
  for (int e = 0; e < 4; ++e) r[e] = (qq[2 * e] & 0xffffu) | (qq[2 * e + 1] << 16);
  *(u32x4*)(ws + OFF_X + off) = r;
#pragma unroll
  for (int e = 0; e < 4; ++e) r[e] = (qq[2 * e] >> 16) | (qq[2 * e + 1] & 0xffff0000u);
  *(u32x4*)(ws + OFF_X + off + 64) = r;
#pragma unroll
  for (int e = 0; e < 4; ++e) r[e] = pk2(g0[2 * e], g0[2 * e + 1]);
  *(u32x4*)(ws + OFF_G + off) = r;
#pragma unroll
  for (int e = 0; e < 4; ++e) r[e] = pk2(g1[2 * e], g1[2 * e + 1]);
  *(u32x4*)(ws + OFF_G + off + 64) = r;
}

DI void tile_w2(int b, const u16* w2, char* ws) {
  const int idx = b * 256 + threadIdx.x;
  const int fg = idx / 224, dp = idx - fg * 224;
  const int ks = fg >> 1, h = fg & 1, d = 2 * dp;
  u32 r[8];
#pragma unroll
  for (int j = 0; j < 8; ++j) r[j] = *(const u32*)(w2 + (size_t)(16 * ks + 8 * (j >> 2) + 4 * h + (j & 3)) * D + d);
  u32x4 lo, hi;
#pragma unroll
  for (int e = 0; e < 4; ++e) { lo[e] = (r[2 * e] & 0xffffu) | (r[2 * e + 1] << 16); hi[e] = (r[2 * e] >> 16) | (r[2 * e + 1] & 0xffff0000u); }
  char* p = ws + OFF_W2 + ((size_t)((d >> 5) * 112 + ks) * 64 + h * 32 + (d & 31)) * 16;
  *(u32x4*)p = lo; *(u32x4*)(p + 16) = hi;
}

DI void tile_w13(int b, const u16* w1, const u16* w3, char* ws, float* lds) {
  const int m = b >= 28 ? 1 : 0, fb = b - 28 * m;
  const u16* w = m ? w3 : w1;
  const int fp = threadIdx.x & 31, slot = threadIdx.x >> 5;
  const int f = fb * 64 + 2 * fp, ft = f >> 4, row = m * 16 + (f & 15);
  u32 r[7][8];
#pragma unroll
  for (int it = 0; it < 7; ++it)
#pragma unroll
    for (int j = 0; j < 8; ++j) r[it][j] = *(const u32*)(w + (size_t)(8 * (slot + 8 * it) + j) * F + f);
  float s0 = 0.f, s1 = 0.f;
#pragma unroll
  for (int it = 0; it < 7; ++it) {
    const int oc = slot + 8 * it;
    u32x4 lo, hi;
#pragma unroll
    for (int e = 0; e < 4; ++e) { lo[e] = (r[it][2 * e] & 0xffffu) | (r[it][2 * e + 1] << 16); hi[e] = (r[it][2 * e] >> 16) | (r[it][2 * e + 1] & 0xffff0000u); }
#pragma unroll
    for (int j = 0; j < 8; ++j) { s0 += bf_lo(r[it][j]); s1 += bf_hi(r[it][j]); }
    char* p = ws + OFF_W13 + ((size_t)(ft * NKA + (oc >> 1)) * 64 + (oc & 1) * 32 + row) * 16;
    *(u32x4*)p = lo; *(u32x4*)(p + 16) = hi;
  }
  lds[slot * 64 + 2 * fp] = s0; lds[slot * 64 + 2 * fp + 1] = s1;
  __syncthreads();
  if (threadIdx.x < 128) {
    const int fl = threadIdx.x & 63, h = threadIdx.x >> 6;
    float cs = 0.f;
#pragma unroll
    for (int sl = 0; sl < 8; ++sl) cs += lds[sl * 64 + fl];
    const float c0 = rbf(cs), c1 = rbf(cs - c0), c2 = rbf(cs - c0 - c1);
    u32x4 o4 = {0u, 0u, 0u, 0u};
    if (h == 0) { o4[0] = pk2(c0, c1); o4[1] = pk2(c2, 0.f); }
    const int ff = fb * 64 + fl;
    *(u32x4*)(ws + OFF_W13 + ((size_t)((ff >> 4) * NKA + 28) * 64 + h * 32 + m * 16 + (ff & 15)) * 16) = o4;
  }
}

__global__ __launch_bounds__(256) void k0_prep(const u16* q, const u16* k, const u16* v, const u16* shk,
                                               const u16* w1, const u16* w3, const u16* w2, char* ws) {
  __shared__ float lds[512];
  const int b = blockIdx.x;
  if (b < 256) prep_tile(b, q, k, v, shk, ws, lds);
  else if (b < 256 + 56) tile_w13(b - 256, w1, w3, ws, lds);
  else tile_w2(b - 312, w2, ws);
}

constexpr int KC_STRIDE = 4416;
constexpr int L1_KC = 0;
constexpr int L1_U = 2 * 4 * KC_STRIDE;
constexpr int U_STRIDE = 286 * 16;
constexpr int L1_P = L1_U + 2 * U_STRIDE;
constexpr int L1_T = L1_P + 4 * 8192;
constexpr int L1_SIZE = L1_T + 8192;

__global__ __launch_bounds__(256) void k1_conv(const u16* ssk, const u16* ssd, char* ws) {
  __shared__ __attribute__((aligned(16))) char lds[L1_SIZE];
  const int tid = threadIdx.x, lane = tid & 63, w = tid >> 6;
  const int b = blockIdx.x, xcd = b & 7, bi = b >> 3;
  const int ch0 = ((xcd * 7 + (bi >> 2)) * 4 + (bi & 3)) * 2;
  u32x4 uin[2], kin[2]; u32x2 knx[2];
#pragma unroll
  for (int rd = 0; rd < 2; ++rd)
    uin[rd] = *(const u32x4*)(ws + OFF_U + ((size_t)((tid >> 3) + 32 * rd) * D + ch0) * 64 + (tid & 7) * 16);
#pragma unroll
  for (int c = 0; c < 2; ++c) {
    const u16* kp = ssk + (size_t)(ch0 + c) * S + tid * 8;
    kin[c] = *(const u32x4*)kp;
    u32x2 nx = *(const u32x2*)(kp + (tid < 255 ? 8 : 0));
    knx[c] = nx;
  }
  u32x2 xin[2][2], gin[2][2];
#pragma unroll
  for (int it = 0; it < 2; ++it)
#pragma unroll
    for (int c = 0; c < 2; ++c) {
      const int n = 4 * (tid + 256 * it);
      const size_t off = ((size_t)(n >> 5) * D + ch0 + c) * 64 + (n & 31) * 2;
      xin[it][c] = *(const u32x2*)(ws + OFF_X + off);
      gin[it][c] = *(const u32x2*)(ws + OFF_G + off);
    }
  const u32 dd = *(const u32*)(ssd + ch0);
  if (tid < 60) *(u32x4*)(lds + L1_U + (tid >= 30 ? U_STRIDE + (tid - 30) * 16 : tid * 16)) = u32x4{0u, 0u, 0u, 0u};
  if (tid >= 64 && tid < 72) *(u32x4*)(lds + L1_KC + (tid - 64) * KC_STRIDE) = u32x4{0u, 0u, 0u, 0u};
#pragma unroll
  for (int c = 0; c < 2; ++c) {
    const u32 d0 = kin[c][0], d1 = kin[c][1], d2 = kin[c][2], d3 = kin[c][3], d4 = knx[c][0], d5 = knx[c][1];
    const u32 e0 = __builtin_amdgcn_alignbit(d1, d0, 16), e1 = __builtin_amdgcn_alignbit(d2, d1, 16), e2 = __builtin_amdgcn_alignbit(d3, d2, 16),
              e3 = __builtin_amdgcn_alignbit(d4, d3, 16), e4 = __builtin_amdgcn_alignbit(d5, d4, 16);
    char* p = lds + L1_KC + c * 4 * KC_STRIDE + (tid + 2) * 16;
    *(u32x4*)(p) = u32x4{d0, d1, d2, d3};
    *(u32x4*)(p + KC_STRIDE) = u32x4{e0, e1, e2, e3};
    *(u32x4*)(p + 2 * KC_STRIDE) = u32x4{d1, d2, d3, d4};
    *(u32x4*)(p + 3 * KC_STRIDE) = u32x4{e1, e2, e3, e4};
    if (tid == 0) {
      char* z = lds + L1_KC + c * 4 * KC_STRIDE + 16;
      *(u32x4*)(z) = u32x4{0u, 0u, 0u, 0u};
      *(u32x4*)(z + KC_STRIDE) = u32x4{0u, 0u, 0u, d0 << 16};
      *(u32x4*)(z + 2 * KC_STRIDE) = u32x4{0u, 0u, 0u, d0};
      *(u32x4*)(z + 3 * KC_STRIDE) = u32x4{0u, 0u, d0 << 16, e0};
    }
  }
#pragma unroll
  for (int rd = 0; rd < 2; ++rd)
    *(u32x4*)(lds + L1_U + ((tid >> 2) & 1) * U_STRIDE + (30 + ((tid >> 3) + 32 * rd) * 4 + (tid & 3)) * 16) = uin[rd];
  __syncthreads();
  {
    const int c = w >> 1, p = w & 1, r = lane & 15, g = lane >> 4;
    const int s = (r + 1) & 3;
    const LAS char* ab = (const LAS char*)lds + L1_KC + (c * 4 + s) * KC_STRIDE + 2 * (32 * p + 25 + r - 8 * g - s);
    const char* bb = lds + L1_U + c * U_STRIDE + (4 - 4 * p + 2 * r + g) * 16;
    u32x4 B[32];
    f32x4 acc[8];
#pragma unroll
    for (int t = 0; t < 8; ++t) acc[t] = f32x4{0.f, 0.f, 0.f, 0.f};
#pragma unroll
    for (int st = 0; st < 32; ++st) {
      const int e = 31 - st;
      B[st] = *(const u32x4*)(bb + st * 128);
      const u32x2 alo = *(const volatile LAS u32x2*)(ab + e * 128);
      const u32x2 ahi = *(const volatile LAS u32x2*)(ab + e * 128 + 8);
      const u32x4 A = {alo[0], alo[1], ahi[0], ahi[1]};
#pragma unroll
      for (int t = 7; t >= 0; --t)
        if (4 * t - e + 3 >= 0) acc[t] = mfma16(A, B[4 * t - e + 3], acc[t]);
    }
    char* pb = lds + L1_P + w * 8192 + 64 * r + 16 * (g ^ ((r >> 1) & 3));
#pragma unroll
    for (int t = 0; t < 8; ++t) *(f32x4*)(pb + 1024 * t) = acc[t];
  }
  __syncthreads();
  const float D0 = bf_lo(dd), D1 = bf_hi(dd);
#pragma unroll
  for (int it = 0; it < 2; ++it) {
    const int qd = tid + 256 * it;
    const char* pp = lds + L1_P + 16 * (qd ^ ((qd >> 3) & 3));
    float hv[2][4];
#pragma unroll
    for (int c = 0; c < 2; ++c) {
      const f32x4 y = *(const f32x4*)(pp + (2 * c) * 8192) + *(const f32x4*)(pp + (2 * c + 1) * 8192);
      const u32x2 uu = *(const u32x2*)(lds + L1_U + c * U_STRIDE + (30 + (qd >> 1)) * 16 + 8 - 8 * (qd & 1));
      const float uv[4] = {bf_hi(uu[1]), bf_lo(uu[1]), bf_hi(uu[0]), bf_lo(uu[0])};
      const float xv[4] = {bf_lo(xin[it][c][0]), bf_hi(xin[it][c][0]), bf_lo(xin[it][c][1]), bf_hi(xin[it][c][1])};
      const float gv[4] = {bf_lo(gin[it][c][0]), bf_hi(gin[it][c][0]), bf_lo(gin[it][c][1]), bf_hi(gin[it][c][1])};
      const float Dc = c ? D1 : D0;
#pragma unroll
      for (int i = 0; i < 4; ++i) {
        const float z = rbf(rbf(y[i]) + rbf(uv[i] * Dc));
        hv[c][i] = xv[i] + rbf(gv[i] * z);
      }
    }
    u32x4 o4;
#pragma unroll
    for (int i = 0; i < 4; ++i) o4[i] = pk2(hv[0][i], hv[1][i]);
    *(u32x4*)(lds + L1_T + qd * 16) = o4;
  }
#pragma unroll
  for (int it = 0; it < 2; ++it)
#pragma unroll
    for (int j = 0; j < 4; ++j) {
      const int n = 1024 * it + 256 * w + 64 * j + lane;
      const u32 val = *(const u32*)(lds + L1_T + n * 4);
      *(u32*)(ws + OFF_H + ((size_t)((n >> 5) * 56 + (ch0 >> 3)) * 32 + (n & 31)) * 16 + (ch0 & 7) * 2) = val;
    }
}

constexpr int L2_X = 0;
constexpr int L2_ST = 4 * 28 * 1024;
constexpr int L2_SIZE = L2_ST + 128 * 8;

DI void glds16(const char* gsrc, u32 lds_dst) {
  u32 keep;
  asm volatile("s_mov_b32 %0, m0\n\ts_mov_b32 m0, %2\n\ts_nop 0\n\tglobal_load_lds_dwordx4 %1, off\n\ts_mov_b32 m0, %0"
               : "=&s"(keep) : "v"(gsrc), "s"(lds_dst) : "memory");
}
#define GLOAD_A(dst, ptr, OFFS) asm volatile("global_load_dwordx4 %0, %1, off offset:%2" : "=a"(dst) : "v"(ptr), "i"(OFFS) : "memory")
#define WAITV_A(N, reg) asm volatile("s_waitcnt vmcnt(%1)" : "+a"(reg) : "i"(N) : "memory")

DI void swiglu_store(const f32x16& acc, float rs, char* dst) {
  const float karg = -1.44269504088896340736f * rs, rs2 = rs * rs;
  float hv[8];
#pragma unroll
  for (int i = 0; i < 8; ++i) {
    const float e = __builtin_amdgcn_exp2f(acc[i] * karg);
    const float sg = __builtin_amdgcn_rcpf(1.0f + e);
    hv[i] = (acc[i] * sg) * (acc[i + 8] * rs2);
  }
  u32x4 o4;
#pragma unroll
  for (int e = 0; e < 4; ++e) o4[e] = pk2(hv[2 * e], hv[2 * e + 1]);
  *(u32x4*)dst = o4;
}

__global__ __launch_bounds__(256) void k2_gemm1(char* ws) {
  extern __shared__ __attribute__((aligned(16))) char lds[];
  const int tid = threadIdx.x, lane = tid & 63;
  const int w = __builtin_amdgcn_readfirstlane(tid >> 6);
  const int b = blockIdx.x, xcd = b & 7, bi = b >> 3;
  const int P = (xcd >> 1) * 4 + (bi & 3), G = (xcd & 1) * 7 + (bi >> 2);
  const int ft0 = 8 * G + 2 * w;
  const u32 ldsb = (u32)(size_t)(LAS char*)lds;
  const char* xg = ws + OFF_H + (size_t)(4 * P + w) * 28 * 1024 + lane * 16;
  const char* wg = ws + OFF_W13 + (size_t)ft0 * NKA * 1024 + lane * 16 + 4096;
  u32x4 W[2][NKA];
#pragma unroll
  for (int ks = 0; ks < 28; ++ks) {
    glds16(xg + ks * 1024, ldsb + L2_X + (w * 28 + ks) * 1024);
    GLOAD_A(W[0][ks], wg + (ks >> 3) * 8192, (ks & 7) * 1024 - 4096);
  }
  GLOAD_A(W[0][28], wg + 3 * 8192, 4 * 1024 - 4096);
  const char* wg1 = wg + NKA * 1024;
#pragma unroll
  for (int ks = 0; ks < 6; ++ks) GLOAD_A(W[1][ks], wg1 + (ks >> 3) * 8192, (ks & 7) * 1024 - 4096);
  f32x16 acc0[4], acc1[4];
#pragma unroll
  for (int t = 0; t < 4; ++t)
#pragma unroll
    for (int i = 0; i < 16; ++i) { acc0[t][i] = 0.f; acc1[t][i] = 0.f; }
  float sum = 0.f, sq = 0.f;
  const char* xl = lds + L2_X + lane * 16;
  const bf16x2 ones = {(__bf16)1.0f, (__bf16)1.0f};
#pragma unroll
  for (int gq = 0; gq < 4; ++gq) {
    const int extra = (8 * gq < 23) ? 8 * gq : 23;
    asm volatile("s_waitcnt vmcnt(%0)\n\ts_barrier" ::"i"(63 + extra - 14 * (gq + 1)) : "memory");
#pragma unroll
    for (int kq = 0; kq < 7; ++kq) {
      const int ks = 7 * gq + kq;
      asm volatile("" : "+a"(W[0][ks]));
#pragma unroll
      for (int t = 0; t < 4; ++t) {
        const u32x4 X = *(const u32x4*)(xl + (t * 28 + ks) * 1024);
        acc0[t] = mfma32(W[0][ks], X, acc0[t]);
      }
      const u32x4 Xs = *(const u32x4*)(xl + (w * 28 + ks) * 1024);
#pragma unroll
      for (int e = 0; e < 4; ++e) {
        const u32 xe = Xs[e];
        const bf16x2 xv = __builtin_bit_cast(bf16x2, xe);
        sum = __builtin_amdgcn_fdot2_f32_bf16(xv, ones, sum, false);
        sq = __builtin_amdgcn_fdot2_f32_bf16(xv, xv, sq, false);
      }
    }
#pragma unroll
    for (int kq = 0; kq < 8; ++kq) {
      const int ks = 6 + 8 * gq + kq;
      if (ks < NKA) GLOAD_A(W[1][ks], wg1 + (ks >> 3) * 8192, (ks & 7) * 1024 - 4096);
    }
  }
  sum += __shfl_xor(sum, 32); sq += __shfl_xor(sq, 32);
  {
    const float mean = sum * (1.0f / D);
    const float var = rbf(fmaxf(sq * (1.0f / D) - mean * mean, 0.f));
    const float sd = rbf(sqrtf(rbf(var + 1e-5f)));
    const float nm = -rbf(mean);
    if (lane < 32) *(u32x2*)(lds + L2_ST + (w * 32 + lane) * 8) = u32x2{pk2(nm, nm), __float_as_uint(1.0f / sd)};
  }
  asm volatile("s_waitcnt lgkmcnt(0)\n\ts_barrier" ::: "memory");
  u32x4 MB[4]; float rs[4];
#pragma unroll
  for (int t = 0; t < 4; ++t) {
    const u32x2 st = *(const u32x2*)(lds + L2_ST + (t * 32 + (lane & 31)) * 8);
    const u32 mm = lane < 32 ? st[0] : 0u;
    MB[t] = u32x4{mm, mm & 0xffffu, 0u, 0u};
    rs[t] = __uint_as_float(st[1]);
  }
  WAITV_A(29, W[0][28]);
#pragma unroll
  for (int t = 0; t < 4; ++t) acc0[t] = mfma32(W[0][28], MB[t], acc0[t]);
  char* ag = ws + OFF_A + ((size_t)(4 * P) * NFT + ft0) * 1024 + lane * 16;
#pragma unroll
  for (int hf = 0; hf < 2; ++hf) {
#pragma unroll
    for (int ks = 0; ks < 28; ++ks) {
      if (hf == 0) WAITV_A(28 - ks > 0 ? 28 - ks : 0, W[1][ks]);
#pragma unroll
      for (int t = 2 * hf; t < 2 * hf + 2; ++t) {
        const u32x4 X = *(const u32x4*)(xl + (t * 28 + ks) * 1024);
        acc1[t] = mfma32(W[1][ks], X, acc1[t]);
      }
      if (hf == 1) {
        if (ks == 0) swiglu_store(acc0[0], rs[0], ag + 0 * NFT * 1024);
        if (ks == 5) swiglu_store(acc0[1], rs[1], ag + 1 * NFT * 1024);
        if (ks == 10) swiglu_store(acc0[2], rs[2], ag + 2 * NFT * 1024);
        if (ks == 15) swiglu_store(acc0[3], rs[3], ag + 3 * NFT * 1024);
        if (ks == 20) swiglu_store(acc1[0], rs[0], ag + 0 * NFT * 1024 + 1024);
        if (ks == 24) swiglu_store(acc1[1], rs[1], ag + 1 * NFT * 1024 + 1024);
      }
    }
    if (hf == 0) WAITV_A(0, W[1][28]);
#pragma unroll
    for (int t = 2 * hf; t < 2 * hf + 2; ++t) acc1[t] = mfma32(W[1][28], MB[t], acc1[t]);
  }
  swiglu_store(acc1[2], rs[2], ag + 2 * NFT * 1024 + 1024);
  swiglu_store(acc1[3], rs[3], ag + 3 * NFT * 1024 + 1024);
}

__global__ __launch_bounds__(256) void k3_gemm2(const char* ws, u16* out) {
  __shared__ __attribute__((aligned(16))) char lds[4 * 3 * 4096];
  const int tid = threadIdx.x, lane = tid & 63;
  const int w = __builtin_amdgcn_readfirstlane(tid >> 6);
  const int b = blockIdx.x, xcd = b & 7, bi = b >> 3;
  const int TP = xcd * 4 + (bi & 3), DP = bi >> 2;
  const char* ap = ws + OFF_W2 + ((size_t)(2 * DP) * 112 + 28 * w) * 1024 + lane * 16;
  const char* bp = ws + OFF_A + ((size_t)(2 * TP) * NFT + 28 * w) * 1024 + lane * 16;
  const int fa = w >> 1, fb = w & 1;
  const int T = 2 * TP + fb, dt = 2 * DP + fa;
  u32x2 hres[4];
#pragma unroll
  for (int g = 0; g < 4; ++g)
    hres[g] = *(const u32x2*)(ws + OFF_H + ((size_t)(T * 56 + 4 * dt + g) * 32 + (lane & 31)) * 16 + 8 * (lane >> 5));
  f32x16 acc[2][2];
#pragma unroll
  for (int a = 0; a < 2; ++a)
#pragma unroll
    for (int c = 0; c < 2; ++c)
#pragma unroll
      for (int i = 0; i < 16; ++i) acc[a][c][i] = 0.f;
  constexpr int PF = 14;
  u32x4 A[28][2], B[28][2];
#pragma unroll
  for (int ks = 0; ks < PF; ++ks) {
    A[ks][0] = *(const u32x4*)(ap + ks * 1024); B[ks][0] = *(const u32x4*)(bp + ks * 1024);
    A[ks][1] = *(const u32x4*)(ap + (112 + ks) * 1024); B[ks][1] = *(const u32x4*)(bp + (NFT + ks) * 1024);
  }
#pragma unroll
  for (int ks = 0; ks < 28; ++ks) {
#pragma unroll
    for (int a = 0; a < 2; ++a)
#pragma unroll
      for (int c = 0; c < 2; ++c) acc[a][c] = mfma32(A[ks][a], B[ks][c], acc[a][c]);
    if (ks + PF < 28) {
      const int kn = ks + PF;
      A[kn][0] = *(const u32x4*)(ap + kn * 1024); B[kn][0] = *(const u32x4*)(bp + kn * 1024);
      A[kn][1] = *(const u32x4*)(ap + (112 + kn) * 1024); B[kn][1] = *(const u32x4*)(bp + (NFT + kn) * 1024);
    }
    __builtin_amdgcn_sched_barrier(0);
  }
#pragma unroll
  for (int j = 0; j < 4; ++j) {
    if (j != w) {
      char* p = lds + (j * 3 + ((w - j - 1) & 3)) * 4096 + lane * 16;
#pragma unroll
      for (int g = 0; g < 4; ++g)
        *(f32x4*)(p + g * 1024) = f32x4{acc[j >> 1][j & 1][4 * g], acc[j >> 1][j & 1][4 * g + 1], acc[j >> 1][j & 1][4 * g + 2], acc[j >> 1][j & 1][4 * g + 3]};
    }
  }
  f32x16 own = w == 0 ? acc[0][0] : w == 1 ? acc[0][1] : w == 2 ? acc[1][0] : acc[1][1];
  __syncthreads();
  u16* op = out + (size_t)(T * 32 + (lane & 31)) * D + dt * 32 + 4 * (lane >> 5);
#pragma unroll
  for (int g = 0; g < 4; ++g) {
    f32x4 sacc = {own[4 * g], own[4 * g + 1], own[4 * g + 2], own[4 * g + 3]};
#pragma unroll
    for (int sl = 0; sl < 3; ++sl) sacc += *(const f32x4*)(lds + (w * 3 + sl) * 4096 + g * 1024 + lane * 16);
    u32x2 o2;
    o2[0] = pk2(bf_lo(hres[g][0]) + rbf(sacc[0]), bf_hi(hres[g][0]) + rbf(sacc[1]));
    o2[1] = pk2(bf_lo(hres[g][1]) + rbf(sacc[2]), bf_hi(hres[g][1]) + rbf(sacc[3]));
    *(u32x2*)(op + 8 * g) = o2;
  }
}

extern "C" void kernel_launch(void* const* d_in, const int* in_sizes, int n_in, void* d_out, int out_size,
                              void* d_ws, size_t ws_size, hipStream_t stream) {
  const u16* q = (const u16*)d_in[0];
  const u16* k = (const u16*)d_in[1];
  const u16* v = (const u16*)d_in[2];
  const u16* shk = (const u16*)d_in[3];
  const u16* ssk = (const u16*)d_in[4];
  const u16* ssd = (const u16*)d_in[5];
  const u16* w1 = (const u16*)d_in[7];
  const u16* w3 = (const u16*)d_in[8];
  const u16* w2 = (const u16*)d_in[9];
  char* ws = (char*)d_ws;
  (void)hipFuncSetAttribute((const void*)k2_gemm1, hipFuncAttributeMaxDynamicSharedMemorySize, L2_SIZE);
  k0_prep<<<256 + 56 + 196, 256, 0, stream>>>(q, k, v, shk, w1, w3, w2, ws);
  k1_conv<<<224, 256, 0, stream>>>(ssk, ssd, ws);
  k2_gemm1<<<224, 256, L2_SIZE, stream>>>(ws);
  k3_gemm2<<<224, 256, 0, stream>>>(ws, (u16*)d_out);
}
```

```cpp
#include <hip/hip_runtime.h>
#include <hip/hip_bf16.h>
#include <stdint.h>

typedef unsigned short u16;
typedef unsigned int u32;
typedef u32 u32x2 __attribute__((ext_vector_type(2)));
typedef u32 u32x4 __attribute__((ext_vector_type(4)));
typedef float f32x4 __attribute__((ext_vector_type(4)));
typedef float f32x16 __attribute__((ext_vector_type(16)));
typedef __bf16 bf16x8 __attribute__((ext_vector_type(8)));
typedef __bf16 bf16x2 __attribute__((ext_vector_type(2)));
#define DI __device__ __forceinline__
#define LAS __attribute__((address_space(3)))

constexpr int S = 2048, D = 448, F = 1792;
constexpr int NKS1 = 28;
constexpr int NKA = 29;
constexpr int NFT = 112;
constexpr size_t SZ_SD = (size_t)S * D * 2;
constexpr size_t OFF_U = 0;
constexpr size_t OFF_X = OFF_U + SZ_SD;
constexpr size_t OFF_G = OFF_X + SZ_SD;
constexpr size_t OFF_H = OFF_G + SZ_SD;
constexpr size_t OFF_W13 = OFF_H + SZ_SD;
constexpr size_t OFF_W2 = OFF_W13 + (size_t)NFT * NKA * 1024;
constexpr size_t OFF_A = OFF_W2 + (size_t)14 * 112 * 1024;

#define TSDECL
#define TS(i)
#define TSOUT(kid, n)

DI float bf_lo(u32 v) { return __uint_as_float(v << 16); }
DI float bf_hi(u32 v) { return __uint_as_float(v & 0xffff0000u); }
DI u32 pk2(float lo, float hi) {
  bf16x2 r; r[0] = (__bf16)lo; r[1] = (__bf16)hi; return __builtin_bit_cast(u32, r);
}
DI float rbf(float x) { return (float)(__bf16)x; }
DI f32x16 mfma32(u32x4 a, u32x4 b, f32x16 c) {
  return __builtin_amdgcn_mfma_f32_32x32x16_bf16(__builtin_bit_cast(bf16x8, a), __builtin_bit_cast(bf16x8, b), c, 0, 0, 0);
}
DI f32x4 mfma16(u32x4 a, u32x4 b, f32x4 c) {
  return __builtin_amdgcn_mfma_f32_16x16x32_bf16(__builtin_bit_cast(bf16x8, a), __builtin_bit_cast(bf16x8, b), c, 0, 0, 0);
}

DI void prep_tile(int b, const u16* q, const u16* k, const u16* v, const u16* shk, char* ws, float* lds) {
  const int tid = threadIdx.x, lane = tid & 63, w = tid >> 6;
  const int xcd = b & 7, i = b >> 3;
  const int tb = xcd * 8 + (i >> 2), cq = i & 3;
  const int t0 = tb * 32;
  const int srow = tid >> 3, sj = tid & 7;
  u32x4 sq[7];
  {
    const u32x4* p = (const u32x4*)(q + (size_t)(t0 + srow) * D) + sj;
#pragma unroll
    for (int c = 0; c < 7; ++c) sq[c] = p[8 * c];
  }
  const int o = lane & 3, cp = (lane >> 2) + 16 * w;
  const bool act = cp < 56;
  const int ch = cq * 112 + 2 * (act ? cp : 0);
  const int r0 = t0 + 8 * o;
  u32 kk[11], vv[8], qq[8];
#pragma unroll
  for (int j = 0; j < 11; ++j) {
    int row = r0 - 3 + j;
    u32 val = *(const u32*)(k + (size_t)(row < 0 ? 0 : row) * D + ch);
    kk[j] = row < 0 ? 0u : val;
  }
#pragma unroll
  for (int j = 0; j < 8; ++j) { vv[j] = *(const u32*)(v + (size_t)(r0 + j) * D + ch); qq[j] = *(const u32*)(q + (size_t)(r0 + j) * D + ch); }
  const u32x4 sk = *(const u32x4*)(shk + ch * 4);
  float s = 0.f;
#pragma unroll
  for (int c = 0; c < 7; ++c)
#pragma unroll
    for (int e = 0; e < 4; ++e) s += bf_lo(sq[c][e]) + bf_hi(sq[c][e]);
  s += __shfl_xor(s, 1); s += __shfl_xor(s, 2); s += __shfl_xor(s, 4);
  const float mean = s * (1.0f / D);
  float ss = 0.f;
#pragma unroll
  for (int c = 0; c < 7; ++c)
#pragma unroll
    for (int e = 0; e < 4; ++e) { float a = bf_lo(sq[c][e]) - mean, bb = bf_hi(sq[c][e]) - mean; ss += a * a + bb * bb; }
  ss += __shfl_xor(ss, 1); ss += __shfl_xor(ss, 2); ss += __shfl_xor(ss, 4);
  if (sj == 0) {
    float var = rbf(ss * (1.0f / D));
    lds[2 * srow] = rbf(mean);
    lds[2 * srow + 1] = rbf(sqrtf(rbf(var + 1e-5f)));
  }
  __syncthreads();
  if (act) {
  float u0[8], u1[8], g0[8], g1[8];
  const float a0 = bf_lo(sk[0]), a1 = bf_hi(sk[0]), a2 = bf_lo(sk[1]), a3 = bf_hi(sk[1]);
  const float c0 = bf_lo(sk[2]), c1 = bf_hi(sk[2]), c2 = bf_lo(sk[3]), c3 = bf_hi(sk[3]);
#pragma unroll
  for (int j = 0; j < 8; ++j) {
    float x0 = rbf(bf_lo(kk[j]) * a3);
    x0 = rbf(x0 + rbf(bf_lo(kk[j + 1]) * a2));
    x0 = rbf(x0 + rbf(bf_lo(kk[j + 2]) * a1));
    x0 = rbf(x0 + rbf(bf_lo(kk[j + 3]) * a0));
    float x1 = rbf(bf_hi(kk[j]) * c3);
    x1 = rbf(x1 + rbf(bf_hi(kk[j + 1]) * c2));
    x1 = rbf(x1 + rbf(bf_hi(kk[j + 2]) * c1));
    x1 = rbf(x1 + rbf(bf_hi(kk[j + 3]) * c0));
    u0[j] = x0 * bf_lo(vv[j]); u1[j] = x1 * bf_hi(vv[j]);
    const float m = lds[2 * (8 * o + j)], sd = lds[2 * (8 * o + j) + 1];
    g0[j] = rbf(bf_lo(qq[j]) - m) / sd; g1[j] = rbf(bf_hi(qq[j]) - m) / sd;
  }
  const size_t off = ((size_t)(tb * D + ch) * 32 + 8 * o) * 2;
  u32x4 r;
#pragma unroll
  for (int e = 0; e < 4; ++e) r[e] = pk2(u0[7 - 2 * e], u0[6 - 2 * e]);
  *(u32x4*)(ws + OFF_U + off) = r;
#pragma unroll
  for (int e = 0; e < 4; ++e) r[e] = pk2(u1[7 - 2 * e], u1[6 - 2 * e]);
  *(u32x4*)(ws + OFF_U + off + 64) = r;
#pragma unroll
  for (int e = 0; e < 4; ++e) r[e] = (qq[2 * e] & 0xffffu) | (qq[2 * e + 1] << 16);
  *(u32x4*)(ws + OFF_X + off) = r;
#pragma unroll
  for (int e = 0; e < 4; ++e) r[e] = (qq[2 * e] >> 16) | (qq[2 * e + 1] & 0xffff0000u);
  *(u32x4*)(ws + OFF_X + off + 64) = r;
#pragma unroll
  for (int e = 0; e < 4; ++e) r[e] = pk2(g0[2 * e], g0[2 * e + 1]);
  *(u32x4*)(ws + OFF_G + off) = r;
#pragma unroll
  for (int e = 0; e < 4; ++e) r[e] = pk2(g1[2 * e], g1[2 * e + 1]);
  *(u32x4*)(ws + OFF_G + off + 64) = r;
  }
}

DI void tile_w2(int b, const u16* w2, char* ws) {
  const int idx = b * 256 + threadIdx.x;
  const int fg = idx / 224, dp = idx - fg * 224;
  const int ks = fg >> 1, h = fg & 1, d = 2 * dp;
  u32 r[8];
#pragma unroll
  for (int j = 0; j < 8; ++j) r[j] = *(const u32*)(w2 + (size_t)(16 * ks + 8 * (j >> 2) + 4 * h + (j & 3)) * D + d);
  u32x4 lo, hi;
#pragma unroll
  for (int e = 0; e < 4; ++e) { lo[e] = (r[2 * e] & 0xffffu) | (r[2 * e + 1] << 16); hi[e] = (r[2 * e] >> 16) | (r[2 * e + 1] & 0xffff0000u); }
  char* p = ws + OFF_W2 + ((size_t)((d >> 5) * 112 + ks) * 64 + h * 32 + (d & 31)) * 16;
  *(u32x4*)p = lo; *(u32x4*)(p + 16) = hi;
}

DI void tile_w13(int b, const u16* w1, const u16* w3, char* ws, float* lds) {
  const int m = b >= 28 ? 1 : 0, fb = b - 28 * m;
  const u16* w = m ? w3 : w1;
  const int fp = threadIdx.x & 31, slot = threadIdx.x >> 5;
  const int f = fb * 64 + 2 * fp, ft = f >> 4, row = m * 16 + (f & 15);
  u32 r[7][8];
#pragma unroll
  for (int it = 0; it < 7; ++it)
#pragma unroll
    for (int j = 0; j < 8; ++j) r[it][j] = *(const u32*)(w + (size_t)(8 * (slot + 8 * it) + j) * F + f);
  float s0 = 0.f, s1 = 0.f;
#pragma unroll
  for (int it = 0; it < 7; ++it) {
    const int oc = slot + 8 * it;
    u32x4 lo, hi;
#pragma unroll
    for (int e = 0; e < 4; ++e) { lo[e] = (r[it][2 * e] & 0xffffu) | (r[it][2 * e + 1] << 16); hi[e] = (r[it][2 * e] >> 16) | (r[it][2 * e + 1] & 0xffff0000u); }
#pragma unroll
    for (int j = 0; j < 8; ++j) { s0 += bf_lo(r[it][j]); s1 += bf_hi(r[it][j]); }
    char* p = ws + OFF_W13 + ((size_t)(ft * NKA + (oc >> 1)) * 64 + (oc & 1) * 32 + row) * 16;
    *(u32x4*)p = lo; *(u32x4*)(p + 16) = hi;
  }
  lds[slot * 64 + 2 * fp] = s0; lds[slot * 64 + 2 * fp + 1] = s1;
  __syncthreads();
  if (threadIdx.x < 128) {
    const int fl = threadIdx.x & 63, h = threadIdx.x >> 6;
    float cs = 0.f;
#pragma unroll
    for (int sl = 0; sl < 8; ++sl) cs += lds[sl * 64 + fl];
    const float c0 = rbf(cs), c1 = rbf(cs - c0), c2 = rbf(cs - c0 - c1);
    u32x4 o4 = {0u, 0u, 0u, 0u};
    if (h == 0) { o4[0] = pk2(c0, c1); o4[1] = pk2(c2, 0.f); }
    const int ff = fb * 64 + fl;
    *(u32x4*)(ws + OFF_W13 + ((size_t)((ff >> 4) * NKA + 28) * 64 + h * 32 + m * 16 + (ff & 15)) * 16) = o4;
  }
}

__global__ __launch_bounds__(256) void k0_prep(const u16* q, const u16* k, const u16* v, const u16* shk,
                                               const u16* w1, const u16* w3, const u16* w2, char* ws) {
  __shared__ float lds[512];
  const int b = blockIdx.x;
  TSDECL; TS(0);
  if (b < 256) prep_tile(b, q, k, v, shk, ws, lds);
  else if (b < 256 + 56) tile_w13(b - 256, w1, w3, ws, lds);
  else tile_w2(b - 312, w2, ws);
  TS(1);
  asm volatile("s_waitcnt vmcnt(0)" ::: "memory");
  TS(2); TSOUT(0, 3);
}

constexpr int KC_STRIDE = 4416;
constexpr int L1_KC = 0;
constexpr int L1_U = 2 * 4 * KC_STRIDE;
constexpr int U_STRIDE = 286 * 16;
constexpr int L1_P = L1_U + 2 * U_STRIDE;
constexpr int L1_T = L1_P + 4 * 8192;
constexpr int L1_SIZE = L1_T + 8192;

__global__ __launch_bounds__(256) void k1_conv(const u16* ssk, const u16* ssd, char* ws) {
  __shared__ __attribute__((aligned(16))) char lds[L1_SIZE];
  const int tid = threadIdx.x, lane = tid & 63, w = tid >> 6;
  const int b = blockIdx.x, xcd = b & 7, bi = b >> 3;
  const int ch0 = ((xcd * 7 + (bi >> 2)) * 4 + (bi & 3)) * 2;
  TSDECL; TS(0);
  u32x4 uin[2], kin[2]; u32x2 knx[2];
#pragma unroll
  for (int rd = 0; rd < 2; ++rd)
    uin[rd] = *(const u32x4*)(ws + OFF_U + ((size_t)((tid >> 3) + 32 * rd) * D + ch0) * 64 + (tid & 7) * 16);
#pragma unroll
  for (int c = 0; c < 2; ++c) {
    const u16* kp = ssk + (size_t)(ch0 + c) * S + tid * 8;
    kin[c] = *(const u32x4*)kp;
    u32x2 nx = *(const u32x2*)(kp + (tid < 255 ? 8 : 0));
    knx[c] = nx;
  }
  u32x2 xin[2][2], gin[2][2];
#pragma unroll
  for (int it = 0; it < 2; ++it)
#pragma unroll
    for (int c = 0; c < 2; ++c) {
      const int n = 4 * (tid + 256 * it);
      const size_t off = ((size_t)(n >> 5) * D + ch0 + c) * 64 + (n & 31) * 2;
      xin[it][c] = *(const u32x2*)(ws + OFF_X + off);
      gin[it][c] = *(const u32x2*)(ws + OFF_G + off);
    }
  const u32 dd = *(const u32*)(ssd + ch0);
  if (tid < 60) *(u32x4*)(lds + L1_U + (tid >= 30 ? U_STRIDE + (tid - 30) * 16 : tid * 16)) = u32x4{0u, 0u, 0u, 0u};
  if (tid >= 64 && tid < 72) *(u32x4*)(lds + L1_KC + (tid - 64) * KC_STRIDE) = u32x4{0u, 0u, 0u, 0u};
#pragma unroll
  for (int c = 0; c < 2; ++c) {
    const u32 d0 = kin[c][0], d1 = kin[c][1], d2 = kin[c][2], d3 = kin[c][3], d4 = knx[c][0], d5 = knx[c][1];
    const u32 e0 = __builtin_amdgcn_alignbit(d1, d0, 16), e1 = __builtin_amdgcn_alignbit(d2, d1, 16), e2 = __builtin_amdgcn_alignbit(d3, d2, 16),
              e3 = __builtin_amdgcn_alignbit(d4, d3, 16), e4 = __builtin_amdgcn_alignbit(d5, d4, 16);
    char* p = lds + L1_KC + c * 4 * KC_STRIDE + (tid + 2) * 16;
    *(u32x4*)(p) = u32x4{d0, d1, d2, d3};
    *(u32x4*)(p + KC_STRIDE) = u32x4{e0, e1, e2, e3};
    *(u32x4*)(p + 2 * KC_STRIDE) = u32x4{d1, d2, d3, d4};
    *(u32x4*)(p + 3 * KC_STRIDE) = u32x4{e1, e2, e3, e4};
    if (tid == 0) {
      char* z = lds + L1_KC + c * 4 * KC_STRIDE + 16;
      *(u32x4*)(z) = u32x4{0u, 0u, 0u, 0u};
      *(u32x4*)(z + KC_STRIDE) = u32x4{0u, 0u, 0u, d0 << 16};
      *(u32x4*)(z + 2 * KC_STRIDE) = u32x4{0u, 0u, 0u, d0};
      *(u32x4*)(z + 3 * KC_STRIDE) = u32x4{0u, 0u, d0 << 16, e0};
    }
  }
#pragma unroll
  for (int rd = 0; rd < 2; ++rd)
    *(u32x4*)(lds + L1_U + ((tid >> 2) & 1) * U_STRIDE + (30 + ((tid >> 3) + 32 * rd) * 4 + (tid & 3)) * 16) = uin[rd];
  TS(1);
  __syncthreads();
  TS(2);
  {
    const int c = w >> 1, p = w & 1, r = lane & 15, g = lane >> 4;
    const int s = (r + 1) & 3;
    const LAS char* ab = (const LAS char*)lds + L1_KC + (c * 4 + s) * KC_STRIDE + 2 * (32 * p + 25 + r - 8 * g - s);
    const char* bb = lds + L1_U + c * U_STRIDE + (4 - 4 * p + 2 * r + g) * 16;
    u32x4 B[32];
    f32x4 acc[8];
#pragma unroll
    for (int t = 0; t < 8; ++t) acc[t] = f32x4{0.f, 0.f, 0.f, 0.f};
#pragma unroll
    for (int st = 0; st < 32; ++st) {
      const int e = 31 - st;
      B[st] = *(const u32x4*)(bb + st * 128);
      const u32x2 alo = *(const volatile LAS u32x2*)(ab + e * 128);
      const u32x2 ahi = *(const volatile LAS u32x2*)(ab + e * 128 + 8);
      const u32x4 A = {alo[0], alo[1], ahi[0], ahi[1]};
#pragma unroll
      for (int t = 7; t >= 0; --t)
        if (4 * t - e + 3 >= 0) acc[t] = mfma16(A, B[4 * t - e + 3], acc[t]);
    }
    char* pb = lds + L1_P + w * 8192 + 64 * r + 16 * (g ^ ((r >> 1) & 3));
#pragma unroll
    for (int t = 0; t < 8; ++t) *(f32x4*)(pb + 1024 * t) = acc[t];
  }
  TS(3);
  __syncthreads();
  TS(4);
  const float D0 = bf_lo(dd), D1 = bf_hi(dd);
#pragma unroll
  for (int it = 0; it < 2; ++it) {
    const int qd = tid + 256 * it;
    const char* pp = lds + L1_P + 16 * (qd ^ ((qd >> 3) & 3));
    float hv[2][4];
#pragma unroll
    for (int c = 0; c < 2; ++c) {
      const f32x4 y = *(const f32x4*)(pp + (2 * c) * 8192) + *(const f32x4*)(pp + (2 * c + 1) * 8192);
      const u32x2 uu = *(const u32x2*)(lds + L1_U + c * U_STRIDE + (30 + (qd >> 1)) * 16 + 8 - 8 * (qd & 1));
      const float uv[4] = {bf_hi(uu[1]), bf_lo(uu[1]), bf_hi(uu[0]), bf_lo(uu[0])};
      const float xv[4] = {bf_lo(xin[it][c][0]), bf_hi(xin[it][c][0]), bf_lo(xin[it][c][1]), bf_hi(xin[it][c][1])};
      const float gv[4] = {bf_lo(gin[it][c][0]), bf_hi(gin[it][c][0]), bf_lo(gin[it][c][1]), bf_hi(gin[it][c][1])};
      const float Dc = c ? D1 : D0;
#pragma unroll
      for (int i = 0; i < 4; ++i) {
        const float z = rbf(rbf(y[i]) + rbf(uv[i] * Dc));
        hv[c][i] = xv[i] + rbf(gv[i] * z);
      }
    }
    u32x4 o4;
#pragma unroll
    for (int i = 0; i < 4; ++i) o4[i] = pk2(hv[0][i], hv[1][i]);
    *(u32x4*)(lds + L1_T + qd * 16) = o4;
  }
#pragma unroll
  for (int it = 0; it < 2; ++it)
#pragma unroll
    for (int j = 0; j < 4; ++j) {
      const int n = 1024 * it + 256 * w + 64 * j + lane;
      const u32 val = *(const u32*)(lds + L1_T + n * 4);
      *(u32*)(ws + OFF_H + ((size_t)((n >> 5) * 56 + (ch0 >> 3)) * 32 + (n & 31)) * 16 + (ch0 & 7) * 2) = val;
    }
  TS(5);
  asm volatile("s_waitcnt vmcnt(0)" ::: "memory");
  TS(6); TSOUT(1, 7);
}

constexpr int L2_X = 0;
constexpr int L2_ST = 4 * 28 * 1024;
constexpr int L2_SIZE = L2_ST + 128 * 8;

DI void glds16(const char* gsrc, u32 lds_dst) {
  u32 keep;
  asm volatile("s_mov_b32 %0, m0\n\ts_mov_b32 m0, %2\n\ts_nop 0\n\tglobal_load_lds_dwordx4 %1, off\n\ts_mov_b32 m0, %0"
               : "=&s"(keep) : "v"(gsrc), "s"(lds_dst) : "memory");
}
#define GLOAD_A(dst, ptr, OFFS) asm volatile("global_load_dwordx4 %0, %1, off offset:%2" : "=a"(dst) : "v"(ptr), "i"(OFFS) : "memory")
#define WAITV_A(N, reg) asm volatile("s_waitcnt vmcnt(%1)" : "+a"(reg) : "i"(N) : "memory")

DI void swiglu_store(const f32x16& acc, float rs, char* dst) {
  const float karg = -1.44269504088896340736f * rs, rs2 = rs * rs;
  float hv[8];
#pragma unroll
  for (int i = 0; i < 8; ++i) {
    const float e = __builtin_amdgcn_exp2f(acc[i] * karg);
    const float sg = __builtin_amdgcn_rcpf(1.0f + e);
    hv[i] = (acc[i] * sg) * (acc[i + 8] * rs2);
  }
  u32x4 o4;
#pragma unroll
  for (int e = 0; e < 4; ++e) o4[e] = pk2(hv[2 * e], hv[2 * e + 1]);
  *(u32x4*)dst = o4;
}

__global__ __launch_bounds__(256) void k2_gemm1(char* ws) {
  extern __shared__ __attribute__((aligned(16))) char lds[];
  const int tid = threadIdx.x, lane = tid & 63;
  const int w = __builtin_amdgcn_readfirstlane(tid >> 6);
  const int b = blockIdx.x, xcd = b & 7, bi = b >> 3;
  const int P = (xcd >> 1) * 4 + (bi & 3), G = (xcd & 1) * 7 + (bi >> 2);
  const int ft0 = 8 * G + 2 * w;
  TSDECL; TS(0);
  const u32 ldsb = (u32)(size_t)(LAS char*)lds;
  const char* xg = ws + OFF_H + (size_t)(4 * P + w) * 28 * 1024 + lane * 16;
  const char* wg = ws + OFF_W13 + (size_t)ft0 * NKA * 1024 + lane * 16 + 4096;
  u32x4 W[2][NKA];
#pragma unroll
  for (int ks = 0; ks < 28; ++ks) {
    glds16(xg + ks * 1024, ldsb + L2_X + (w * 28 + ks) * 1024);
    GLOAD_A(W[0][ks], wg + (ks >> 3) * 8192, (ks & 7) * 1024 - 4096);
  }
  GLOAD_A(W[0][28], wg + 3 * 8192, 4 * 1024 - 4096);
  const char* wg1 = wg + NKA * 1024;
#pragma unroll
  for (int ks = 0; ks < 6; ++ks) GLOAD_A(W[1][ks], wg1 + (ks >> 3) * 8192, (ks & 7) * 1024 - 4096);
  TS(1);
  f32x16 acc0[4], acc1[4];
#pragma unroll
  for (int t = 0; t < 4; ++t)
#pragma unroll
    for (int i = 0; i < 16; ++i) { acc0[t][i] = 0.f; acc1[t][i] = 0.f; }
  float sum = 0.f, sq = 0.f;
  const char* xl = lds + L2_X + lane * 16;
  const bf16x2 ones = {(__bf16)1.0f, (__bf16)1.0f};
#pragma unroll
  for (int gq = 0; gq < 4; ++gq) {
    const int extra = (8 * gq < 23) ? 8 * gq : 23;
    asm volatile("s_waitcnt vmcnt(%0)\n\ts_barrier" ::"i"(63 + extra - 14 * (gq + 1)) : "memory");
    if (gq == 0) TS(2);
#pragma unroll
    for (int kq = 0; kq < 7; ++kq) {
      const int ks = 7 * gq + kq;
      asm volatile("" : "+a"(W[0][ks]));
#pragma unroll
      for (int t = 0; t < 4; ++t) {
        const u32x4 X = *(const u32x4*)(xl + (t * 28 + ks) * 1024);
        acc0[t] = mfma32(W[0][ks], X, acc0[t]);
      }
      const u32x4 Xs = *(const u32x4*)(xl + (w * 28 + ks) * 1024);
#pragma unroll
      for (int e = 0; e < 4; ++e) {
        const u32 xe = Xs[e];
        const bf16x2 xv = __builtin_bit_cast(bf16x2, xe);
        sum = __builtin_amdgcn_fdot2_f32_bf16(xv, ones, sum, false);
        sq = __builtin_amdgcn_fdot2_f32_bf16(xv, xv, sq, false);
      }
    }
#pragma unroll
    for (int kq = 0; kq < 8; ++kq) {
      const int ks = 6 + 8 * gq + kq;
      if (ks < NKA) GLOAD_A(W[1][ks], wg1 + (ks >> 3) * 8192, (ks & 7) * 1024 - 4096);
    }
  }
  TS(3);
  sum += __shfl_xor(sum, 32); sq += __shfl_xor(sq, 32);
  {
    const float mean = sum * (1.0f / D);
    const float var = rbf(fmaxf(sq * (1.0f / D) - mean * mean, 0.f));
    const float sd = rbf(sqrtf(rbf(var + 1e-5f)));
    const float nm = -rbf(mean);
    if (lane < 32) *(u32x2*)(lds + L2_ST + (w * 32 + lane) * 8) = u32x2{pk2(nm, nm), __float_as_uint(1.0f / sd)};
  }
  asm volatile("s_waitcnt lgkmcnt(0)\n\ts_barrier" ::: "memory");
  u32x4 MB[4]; float rs[4];
#pragma unroll
  for (int t = 0; t < 4; ++t) {
    const u32x2 st = *(const u32x2*)(lds + L2_ST + (t * 32 + (lane & 31)) * 8);
    const u32 mm = lane < 32 ? st[0] : 0u;
    MB[t] = u32x4{mm, mm & 0xffffu, 0u, 0u};
    rs[t] = __uint_as_float(st[1]);
  }
  WAITV_A(29, W[0][28]);
#pragma unroll
  for (int t = 0; t < 4; ++t) acc0[t] = mfma32(W[0][28], MB[t], acc0[t]);
  char* ag = ws + OFF_A + ((size_t)(4 * P) * NFT + ft0) * 1024 + lane * 16;
  TS(4);
#pragma unroll
  for (int hf = 0; hf < 2; ++hf) {
#pragma unroll
    for (int ks = 0; ks < 28; ++ks) {
      if (hf == 0) WAITV_A(28 - ks > 0 ? 28 - ks : 0, W[1][ks]);
#pragma unroll
      for (int t = 2 * hf; t < 2 * hf + 2; ++t) {
        const u32x4 X = *(const u32x4*)(xl + (t * 28 + ks) * 1024);
        acc1[t] = mfma32(W[1][ks], X, acc1[t]);
      }
      if (hf == 1) {
        if (ks == 0) swiglu_store(acc0[0], rs[0], ag + 0 * NFT * 1024);
        if (ks == 5) swiglu_store(acc0[1], rs[1], ag + 1 * NFT * 1024);
        if (ks == 10) swiglu_store(acc0[2], rs[2], ag + 2 * NFT * 1024);
        if (ks == 15) swiglu_store(acc0[3], rs[3], ag + 3 * NFT * 1024);
        if (ks == 20) swiglu_store(acc1[0], rs[0], ag + 0 * NFT * 1024 + 1024);
        if (ks == 24) swiglu_store(acc1[1], rs[1], ag + 1 * NFT * 1024 + 1024);
      }
    }
    if (hf == 0) WAITV_A(0, W[1][28]);
    if (hf == 0) TS(5);
#pragma unroll
    for (int t = 2 * hf; t < 2 * hf + 2; ++t) acc1[t] = mfma32(W[1][28], MB[t], acc1[t]);
  }
  swiglu_store(acc1[2], rs[2], ag + 2 * NFT * 1024 + 1024);
  swiglu_store(acc1[3], rs[3], ag + 3 * NFT * 1024 + 1024);
  TS(6);
  asm volatile("s_waitcnt vmcnt(0)" ::: "memory");
  TS(7); TSOUT(2, 8);
}

__global__ __launch_bounds__(256) void k3_gemm2(const char* ws, u16* out) {
  __shared__ __attribute__((aligned(16))) char lds[4 * 3 * 4096];
  const int tid = threadIdx.x, lane = tid & 63;
  const int w = __builtin_amdgcn_readfirstlane(tid >> 6);
  const int b = blockIdx.x, xcd = b & 7, bi = b >> 3;
  const int TP = xcd * 4 + (bi & 3), DP = bi >> 2;
  TSDECL; TS(0);
  const char* ap = ws + OFF_W2 + ((size_t)(2 * DP) * 112 + 28 * w) * 1024 + lane * 16;
  const char* bp = ws + OFF_A + ((size_t)(2 * TP) * NFT + 28 * w) * 1024 + lane * 16;
  const int fa = w >> 1, fb = w & 1;
  const int T = 2 * TP + fb, dt = 2 * DP + fa;
  u32x2 hres[4];
#pragma unroll
  for (int g = 0; g < 4; ++g)
    hres[g] = *(const u32x2*)(ws + OFF_H + ((size_t)(T * 56 + 4 * dt + g) * 32 + (lane & 31)) * 16 + 8 * (lane >> 5));
  f32x16 acc[2][2];
#pragma unroll
  for (int a = 0; a < 2; ++a)
#pragma unroll
    for (int c = 0; c < 2; ++c)
#pragma unroll
      for (int i = 0; i < 16; ++i) acc[a][c][i] = 0.f;
  constexpr int PF = 14;
  u32x4 A[28][2], B[28][2];
#pragma unroll
  for (int ks = 0; ks < PF; ++ks) {
    A[ks][0] = *(const u32x4*)(ap + ks * 1024); B[ks][0] = *(const u32x4*)(bp + ks * 1024);
    A[ks][1] = *(const u32x4*)(ap + (112 + ks) * 1024); B[ks][1] = *(const u32x4*)(bp + (NFT + ks) * 1024);
  }
  TS(1);
#pragma unroll
  for (int ks = 0; ks < 28; ++ks) {
    if (ks == 1) TS(2);
    if (ks == 14) TS(3);
#pragma unroll
    for (int a = 0; a < 2; ++a)
#pragma unroll
      for (int c = 0; c < 2; ++c) acc[a][c] = mfma32(A[ks][a], B[ks][c], acc[a][c]);
    if (ks + PF < 28) {
      const int kn = ks + PF;
      A[kn][0] = *(const u32x4*)(ap + kn * 1024); B[kn][0] = *(const u32x4*)(bp + kn * 1024);
      A[kn][1] = *(const u32x4*)(ap + (112 + kn) * 1024); B[kn][1] = *(const u32x4*)(bp + (NFT + kn) * 1024);
    }
    __builtin_amdgcn_sched_barrier(0);
  }
  TS(4);
#pragma unroll
  for (int j = 0; j < 4; ++j) {
    if (j != w) {
      char* p = lds + (j * 3 + ((w - j - 1) & 3)) * 4096 + lane * 16;
#pragma unroll
      for (int g = 0; g < 4; ++g)
        *(f32x4*)(p + g * 1024) = f32x4{acc[j >> 1][j & 1][4 * g], acc[j >> 1][j & 1][4 * g + 1], acc[j >> 1][j & 1][4 * g + 2], acc[j >> 1][j & 1][4 * g + 3]};
    }
  }
  f32x16 own = w == 0 ? acc[0][0] : w == 1 ? acc[0][1] : w == 2 ? acc[1][0] : acc[1][1];
  __syncthreads();
  u16* op = out + (size_t)(T * 32 + (lane & 31)) * D + dt * 32 + 4 * (lane >> 5);
#pragma unroll
  for (int g = 0; g < 4; ++g) {
    f32x4 sacc = {own[4 * g], own[4 * g + 1], own[4 * g + 2], own[4 * g + 3]};
#pragma unroll
    for (int sl = 0; sl < 3; ++sl) sacc += *(const f32x4*)(lds + (w * 3 + sl) * 4096 + g * 1024 + lane * 16);
    u32x2 o2;
    o2[0] = pk2(bf_lo(hres[g][0]) + rbf(sacc[0]), bf_hi(hres[g][0]) + rbf(sacc[1]));
    o2[1] = pk2(bf_lo(hres[g][1]) + rbf(sacc[2]), bf_hi(hres[g][1]) + rbf(sacc[3]));
    *(u32x2*)(op + 8 * g) = o2;
  }
  TS(5);
  asm volatile("s_waitcnt vmcnt(0)" ::: "memory");
  TS(6); TSOUT(3, 7);
}

extern "C" void kernel_launch(void* const* d_in, const int* in_sizes, int n_in, void* d_out, int out_size,
                              void* d_ws, size_t ws_size, hipStream_t stream) {
  const u16* q = (const u16*)d_in[0];
  const u16* k = (const u16*)d_in[1];
  const u16* v = (const u16*)d_in[2];
  const u16* shk = (const u16*)d_in[3];
  const u16* ssk = (const u16*)d_in[4];
  const u16* ssd = (const u16*)d_in[5];
  const u16* w1 = (const u16*)d_in[7];
  const u16* w3 = (const u16*)d_in[8];
  const u16* w2 = (const u16*)d_in[9];
  char* ws = (char*)d_ws;
  (void)hipFuncSetAttribute((const void*)k2_gemm1, hipFuncAttributeMaxDynamicSharedMemorySize, L2_SIZE);
  k0_prep<<<256 + 56 + 196, 256, 0, stream>>>(q, k, v, shk, w1, w3, w2, ws);
  k1_conv<<<224, 256, 0, stream>>>(ssk, ssd, ws);
  k2_gemm1<<<224, 256, L2_SIZE, stream>>>(ws);
  k3_gemm2<<<224, 256, 0, stream>>>(ws, (u16*)d_out);
}
```

```cpp
#include <hip/hip_runtime.h>
#include <hip/hip_bf16.h>
#include <stdint.h>

typedef unsigned short u16;
typedef unsigned int u32;
typedef unsigned long long u64;
typedef u32 u32x2 __attribute__((ext_vector_type(2)));
typedef u32 u32x4 __attribute__((ext_vector_type(4)));
typedef short s16x4 __attribute__((ext_vector_type(4)));
typedef float f32x4 __attribute__((ext_vector_type(4)));
typedef float f32x16 __attribute__((ext_vector_type(16)));
typedef __bf16 bf16x8 __attribute__((ext_vector_type(8)));
typedef __bf16 bf16x2 __attribute__((ext_vector_type(2)));
#define DI __device__ __forceinline__
#define LAS __attribute__((address_space(3)))
#define SB() __builtin_amdgcn_sched_barrier(0)
template <int V> struct IC { static constexpr int v = V; };
template <int I, int N, class Fn> DI void sfor(Fn&& f) { if constexpr (I < N) { f(IC<I>{}); sfor<I + 1, N>(f); } }
#define SFOR(var, n) sfor<0, n>([&](auto ic_##var) __attribute__((always_inline)) { constexpr int var = decltype(ic_##var)::v;
#define SEND });

constexpr int S = 2048, D = 448, F = 1792;
constexpr u32 OFF_E = 0;
constexpr u32 OFF_FP = 4096;
constexpr u32 OFF_FA = OFF_FP + 16384;
constexpr u32 OFF_ST = OFF_FA + 16384;
constexpr u32 OFF_S2 = OFF_ST + 16384;
constexpr u32 SZ_SD = S * D * 2;
constexpr u32 OFF_U = OFF_S2 + 32768;
constexpr u32 OFF_X = OFF_U + 2 * SZ_SD;
constexpr u32 OFF_H = OFF_X + 2 * SZ_SD;
constexpr u32 OFF_A = OFF_H + SZ_SD;
constexpr u32 OFF_Q = OFF_A + S * F * 2;
constexpr u32 WS_END = OFF_Q + 16 * 14 * 8 * 2048;
constexpr int KC = 4160;
constexpr int L_F = 0;
constexpr int L_U = 8 * KC;
constexpr int US = 286 * 16;
constexpr int L_WA = 42496;
constexpr int L_WB = 80896;
constexpr int L_WC = 0;
constexpr int L_PE = 42496, L_PL = 64256;
constexpr int L_X0 = 32768, L_X1 = 64512, L_X2 = L_X1 + 28672, L_X3 = L_X2 + 28672;
constexpr int L_PV = 9216, L_PQ = 17408;
constexpr int L_EA = 150528, L_EB = 114688, L_EF = 118784;
constexpr int L_CS = 162816;
constexpr int L_SIZE = 163840;
constexpr int MAXSPIN = 1 << 18;

#define TSDECL
#define TS(i)
#define TSOUT()

DI float bf_lo(u32 v) { return __uint_as_float(v << 16); }
DI float bf_hi(u32 v) { return __uint_as_float(v & 0xffff0000u); }
DI u32 pk2(float lo, float hi) { bf16x2 r; r[0] = (__bf16)lo; r[1] = (__bf16)hi; return __builtin_bit_cast(u32, r); }
DI float rbf(float x) { return (float)(__bf16)x; }
DI u32 plo(u32 a, u32 b) { return __builtin_amdgcn_perm(b, a, 0x05040100u); }
DI u32 phi(u32 a, u32 b) { return __builtin_amdgcn_perm(b, a, 0x07060302u); }
DI f32x16 mfma32(u32x4 a, u32x4 b, f32x16 c) {
  return __builtin_amdgcn_mfma_f32_32x32x16_bf16(__builtin_bit_cast(bf16x8, a), __builtin_bit_cast(bf16x8, b), c, 0, 0, 0);
}
DI f32x4 mfma16(u32x4 a, u32x4 b, f32x4 c) {
  return __builtin_amdgcn_mfma_f32_16x16x32_bf16(__builtin_bit_cast(bf16x8, a), __builtin_bit_cast(bf16x8, b), c, 0, 0, 0);
}
DI float dot2(u32 a, u32 b, float c) { return __builtin_amdgcn_fdot2_f32_bf16(__builtin_bit_cast(bf16x2, a), __builtin_bit_cast(bf16x2, b), c, false); }
DI u32x2 trd(const LAS char* p) { return __builtin_bit_cast(u32x2, __builtin_amdgcn_ds_read_tr16_b64_v4i16((LAS s16x4*)p)); }
DI u32x4 trd2(const LAS char* p, int d) { const u32x2 a = trd(p), b = trd(p + d); return u32x4{a[0], a[1], b[0], b[1]}; }
DI u32 umax3(u32 a, u32 b, u32 c) { const u32 m = a > b ? a : b; return m > c ? m : c; }
DI u32 nop32(u32 x) { return x < 0xfffffffeu ? x : 0xfffffffeu; }
DI bool allz(bool bad) { return __builtin_amdgcn_ballot_w64(bad) == 0; }
DI u32x4 mksrd(const void* p, u32 bytes) { const u64 a = (u64)p; return u32x4{(u32)a, (u32)(a >> 32) & 0xffffu, bytes, 0x00020000u}; }

#define LD1(d, vo, srd, off) asm volatile("buffer_load_dword %0, %1, %2, 0 offen offset:%3" : "=v"(d) : "v"(vo), "s"(srd), "i"(off) : "memory")
#define LD2(d, vo, srd, off) asm volatile("buffer_load_dwordx2 %0, %1, %2, 0 offen offset:%3" : "=v"(d) : "v"(vo), "s"(srd), "i"(off) : "memory")
#define LD4(d, vo, srd, off) asm volatile("buffer_load_dwordx4 %0, %1, %2, 0 offen offset:%3" : "=v"(d) : "v"(vo), "s"(srd), "i"(off) : "memory")
#define LDC1(d, vo, srd) asm volatile("buffer_load_dword %0, %1, %2, 0 offen sc1" : "=v"(d) : "v"(vo), "s"(srd) : "memory")
#define LDC2(d, vo, srd, so) asm volatile("buffer_load_dwordx2 %0, %1, %2, %3 offen sc1" : "=v"(d) : "v"(vo), "s"(srd), "s"(so) : "memory")
#define LDC4(d, vo, srd, so, off) asm volatile("buffer_load_dwordx4 %0, %1, %2, %3 offen offset:%4 sc1" : "=v"(d) : "v"(vo), "s"(srd), "s"(so), "i"(off) : "memory")
#define STC4(v, vo, srd, off) asm volatile("buffer_store_dwordx4 %0, %1, %2, 0 offen offset:%3 sc1\n\ts_nop 1" :: "v"(v), "v"(vo), "s"(srd), "i"(off) : "memory")
#define STF(v, vo, srd) asm volatile("s_mov_b64 exec, 1\n\tbuffer_store_dwordx2 %0, %1, %2, 0 offen sc1\n\ts_mov_b64 exec, -1" :: "v"(v), "v"(vo), "s"(srd) : "memory")
#define DMA(la, vo, srd, so) asm volatile("s_mov_b32 m0, %0\n\ts_nop 0\n\tbuffer_load_dwordx4 %1, %2, %3 offen lds" :: "s"(la), "v"(vo), "s"(srd), "s"(so) : "memory")
#define WAITV(n) asm volatile("s_waitcnt vmcnt(%0)" :: "i"(n) : "memory")
#define TIE(x) asm volatile("" : "+v"(x))
DI void bar() { asm volatile("s_waitcnt lgkmcnt(0)\n\ts_barrier" ::: "memory"); }
DI bool gbad(u32x4 g, u32 tA, u32 tB) { return g[1] != tA || g[3] != tB; }
DI void flagwait(u32x2 f, u32 vo, u32x4 srd, u32 t0, u32 t1) {
  if (!allz(f[0] != t0 || f[1] != t1)) {

#pragma unroll 1
    for (int it = 0; it < MAXSPIN; ++it) {
      __builtin_amdgcn_s_sleep(1);
      LDC2(f, vo, srd, 0); WAITV(0); TIE(f);
      if (allz(f[0] != t0 || f[1] != t1)) break;
    }

  }
}

__global__ __launch_bounds__(512, 2) void fused(const u16* __restrict__ q, const u16* __restrict__ k, const u16* __restrict__ v,
                                                const u16* __restrict__ shk, const u16* __restrict__ ssk, const u16* __restrict__ ssd,
                                                const u16* __restrict__ w1, const u16* __restrict__ w3, const u16* __restrict__ w2,
                                                u16* __restrict__ out, char* ws) {
  extern __shared__ __attribute__((aligned(16))) char lds_[];
  LAS char* lds = (LAS char*)lds_;
  const int tid = threadIdx.x, lane = tid & 63;
  const int w = __builtin_amdgcn_readfirstlane(tid >> 6);
  const int b = blockIdx.x, xcd = b & 7, bi = b >> 3;
  const bool spare = bi >= 28;
  TSDECL; TS(0);
  const u32x4 rs = mksrd(ws, WS_END), rq = mksrd(q, SZ_SD), rk = mksrd(k, SZ_SD), rv = mksrd(v, SZ_SD), rh = mksrd(shk, D * 8),
              rf = mksrd(ssk, SZ_SD), rw = mksrd((w & 1) ? w3 : w1, D * F * 2), r2 = mksrd(w2, D * F * 2);
  const int jc = spare ? 0 : xcd * 28 + bi;
  const int P = (xcd >> 1) * 4 + (bi & 3), G = (xcd & 1) * 7 + (spare ? 0 : bi >> 2);
  const u32 dd = *(const u32*)(ssd + 2 * jc);

  const int tb = xcd * 8 + (bi >> 2), cq = bi & 3;
  const int tq = lane & 7;
  u32x2 e0; u32x4 kin; u32x2 knx;
  const u32 zero = 0;
  LDC2(e0, zero, rs, 0);
  const u32 wvo = (lane >> 4) * (F * 2) + G * 256 + (((lane & 15) ^ ((4 * (w & 1) + (lane >> 4)) << 1)) << 4);
  const u32 wso = (w >> 1) * 4 * (F * 2);
  const u32 fvo = jc * (2 * S * 2) + tid * 16;
  {
    const int c = (lane & 15) ^ w;
    const u32 o = (tb * 32 + 4 * w + (lane >> 4)) * (D * 2) + cq * 224 + c * 16;
    const u32 ov = c < 14 ? o : 0x80000000u, ok = (c < 14 && (tb | w)) ? o - 4 * (D * 2) : 0x80000000u;
    DMA(w * 1024, ok, rk, 0); DMA(L_PV + w * 1024, ov, rv, 0); DMA(L_PQ + w * 1024, ov, rq, 0);
  }
  constexpr int NE = 3;
#define DMAA(i) DMA(L_WA + (i) * 8192 + w * 1024, wvo, rw, wso + (i) * 16 * (F * 2))
#define MKTAG() do { const u64 tg = (((u64)(u32)__builtin_amdgcn_readfirstlane(e0[1]) << 32) | (u32)__builtin_amdgcn_readfirstlane(e0[0])) + 1; \
    t0 = (u32)tg; t1 = (u32)(tg >> 32); tA = t0; tB = t0 ^ t1 ^ 0x9E3779B9u; } while (0)
  u32 t0, t1, tA, tB;
  if (w < 7) {
    const int cp = 8 * w + (lane >> 3);
    u32x4 sk;
    { const u32 so = (cq * 112 + 2 * cp) * 8; LD4(sk, so, rh, 0); }
    LD4(kin, fvo, rf, 0); LD2(knx, fvo, rf, 16);
    if (!spare) {
#pragma unroll
      for (int i = 0; i < NE; ++i) DMAA(i);
      WAITV(NE);
    } else {
      WAITV(0);
    }
    TIE(e0); TIE(sk); TIE(kin); TIE(knx);
    TS(1);
    bar();
    const int c = 2 * w + (lane >> 5);
    const LAS char* pa = lds + tq * 1024 + ((c ^ tq) << 4) + ((lane >> 3) & 3) * 4;
    const LAS char* pb = lds + tq * 1024 + ((c ^ ((tq + 1) & 7)) << 4) + ((lane >> 3) & 3) * 4;
    u32 kk[7], vv[4], qq[4];
#pragma unroll
    for (int j = 0; j < 7; ++j) kk[j] = *(const LAS u32*)((j < 3 ? pa : pb) + (j + 1) * 256);
#pragma unroll
    for (int j = 0; j < 4; ++j) { vv[j] = *(const LAS u32*)(pa + L_PV + j * 256); qq[j] = *(const LAS u32*)(pa + L_PQ + j * 256); }
    MKTAG();
    bar();
    TS(2);
    const u32 s0 = sk[0], s1 = sk[1], s2 = sk[2], s3 = sk[3];
    const float a0 = bf_lo(s0), a1 = bf_hi(s0), a2 = bf_lo(s1), a3 = bf_hi(s1);
    const float c0 = bf_lo(s2), c1 = bf_hi(s2), c2 = bf_lo(s3), c3 = bf_hi(s3);
    float u0[4], u1[4];
#pragma unroll
    for (int j = 0; j < 4; ++j) {
      u0[j] = (bf_lo(kk[j]) * a3 + bf_lo(kk[j + 1]) * a2 + bf_lo(kk[j + 2]) * a1 + bf_lo(kk[j + 3]) * a0) * bf_lo(vv[j]);
      u1[j] = (bf_hi(kk[j]) * c3 + bf_hi(kk[j + 1]) * c2 + bf_hi(kk[j + 2]) * c1 + bf_hi(kk[j + 3]) * c0) * bf_hi(vv[j]);
    }
    const u32 uo = OFF_U + tb * 57344 + (cq * 112 + 2 * cp) * 128 + tq * 16;
    { const u32x4 g = {pk2(u0[3], u0[2]), tA, pk2(u0[1], u0[0]), tB}; STC4(g, uo, rs, 0); }
    { const u32x4 g = {pk2(u1[3], u1[2]), tA, pk2(u1[1], u1[0]), tB}; STC4(g, uo, rs, 128); }
    const u32 xo = uo + (OFF_X - OFF_U);
    { const u32x4 g = {plo(qq[0], qq[1]), tA, plo(qq[2], qq[3]), tB}; STC4(g, xo, rs, 0); }
    { const u32x4 g = {phi(qq[0], qq[1]), tA, phi(qq[2], qq[3]), tB}; STC4(g, xo, rs, 128); }
    { const u32x4 ones = {~0u, ~0u, ~0u, ~0u}; const u32 po = OFF_H + tb * 28672 + cq * 7168 + tid * 16; STC4(ones, po, rs, 0); }
  } else {
    u32x4 sq[7];
    { const u32 o = (tb * 32 + 28 + (lane >> 4)) * (D * 2) + cq * 224 + (lane & 15) * 16; const u32 ok = (lane & 15) < 14 ? o : 0x80000000u; DMA(8192, ok, rk, 0); }
    const u32 vo = (tb * 32 + 8 * cq + (lane >> 3)) * (D * 2) + (lane & 7) * 16;
    LD4(kin, fvo, rf, 0); LD2(knx, fvo, rf, 16);
    LD4(sq[0], vo, rq, 0); LD4(sq[1], vo, rq, 128); LD4(sq[2], vo, rq, 256); LD4(sq[3], vo, rq, 384);
    LD4(sq[4], vo, rq, 512); LD4(sq[5], vo, rq, 640); LD4(sq[6], vo, rq, 768);
    if (!spare) {
#pragma unroll
      for (int i = 0; i < NE; ++i) DMAA(i);
      WAITV(NE + 7);
    } else {
      WAITV(7);
    }
    TIE(e0); TIE(kin); TIE(knx);
    TS(1);
    bar();
    MKTAG();
    bar();
    TS(2);
    if (!spare) WAITV(NE); else WAITV(0);
#pragma unroll
    for (int c = 0; c < 7; ++c) TIE(sq[c]);
    float s = 0.f, ss = 0.f;
#pragma unroll
    for (int c = 0; c < 7; ++c)
#pragma unroll
      for (int e = 0; e < 4; ++e) { const u32 x = sq[c][e]; s = dot2(x, 0x3f803f80u, s); ss = dot2(x, x, ss); }
    s += __shfl_xor(s, 1); ss += __shfl_xor(ss, 1);
    s += __shfl_xor(s, 2); ss += __shfl_xor(ss, 2);
    s += __shfl_xor(s, 4); ss += __shfl_xor(ss, 4);
    const float mean = s * (1.0f / D);
    const float var = rbf(fmaxf(ss * (1.0f / D) - mean * mean, 0.f));
    const u32 st = pk2(mean, sqrtf(rbf(var + 1e-5f)));
    const u32 sa = __shfl(st, (lane & 3) * 16), sb = __shfl(st, (lane & 3) * 16 + 8);
    if (lane < 4) { const u32x4 g = {sa, tA, sb, tB}; const u32 so = OFF_ST + (lane & 1) * 8192 + (tb * 8 + 2 * cq + (lane >> 1)) * 16; STC4(g, so, rs, 0); }
  }
  TS(3);
  const u32 fpo = OFF_FP + (tb * 32 + (w < 7 ? cq * 7 + w : 28 + cq)) * 8;
#define FPFLAG() do { const u32x2 f = {t0, t1}; STF(f, fpo, rs); } while (0)

  if (spare) {

    WAITV(0); FPFLAG();
    const int s = xcd * 4 + (bi - 28);
    const int b3 = w < 4 ? w + 24 : w + 16;
    LAS char* blk = lds + w * 4096;
    const LAS char* xb = blk + (8 * (lane >> 5) + ((lane & 15) >> 2)) * 64 + ((lane >> 4) & 1) * 32 + (lane & 3) * 8;
#pragma unroll 1
    for (int hf = 0; hf < 2; ++hf) {
      const int tbs = s + 32 * hf;
      {
        const u32 fo = OFF_FP + (tbs * 32 + ((lane & 3) == 3 ? b3 : w + 8 * (lane & 3))) * 8;
        u32x2 f; LDC2(f, fo, rs, 0); WAITV(0); TIE(f);
        flagwait(f, fo, rs, t0, t1);
      }
      u32x4 xs[4];
      const u32 xo = OFF_H + (tbs * 28 + w) * 1024 + lane * 16, xo3 = OFF_H + (tbs * 28 + b3) * 1024 + lane * 16;
#pragma unroll 1
      for (int it = 0; it < MAXSPIN; ++it) {
        LDC4(xs[0], xo, rs, 0, 0); LDC4(xs[1], xo, rs, 8192, 0); LDC4(xs[2], xo, rs, 16384, 0); LDC4(xs[3], xo3, rs, 0, 0);
        WAITV(0);
        u32 m = 0;
#pragma unroll
        for (int i = 0; i < 4; ++i) { TIE(xs[i]); m = umax3(m, xs[i][0], xs[i][1]); m = umax3(m, xs[i][2], xs[i][3]); }
        if (allz(m == ~0u)) break;
        __builtin_amdgcn_s_sleep(2);
      }
      if (hf == 0) TS(4); else TS(6);
#pragma unroll
      for (int i = 0; i < 4; ++i) *(LAS u32x4*)(blk + i * 1024 + lane * 16) = xs[i];
      float sm = 0.f, ss = 0.f;
#pragma unroll
      for (int i = 0; i < 4; ++i) {
        u32x4 x = trd2(xb + i * 1024, 256);
        if (i == 3 && w >= 4) x = u32x4{0u, 0u, 0u, 0u};
#pragma unroll
        for (int e = 0; e < 4; ++e) { const u32 xe = x[e]; sm = dot2(xe, 0x3f803f80u, sm); ss = dot2(xe, xe, ss); }
      }
      *(LAS u32x2*)(lds + 32768 + hf * 4096 + (w * 64 + lane) * 8) = u32x2{__float_as_uint(sm), __float_as_uint(ss)};
      bar();
      if (w == 0) {
        float a = 0.f, c = 0.f;
#pragma unroll
        for (int i = 0; i < 8; ++i) {
          const u32x2 p = *(const LAS u32x2*)(lds + 32768 + hf * 4096 + (i * 64 + lane) * 8);
          const u32 p0 = p[0], p1 = p[1];
          a += __uint_as_float(p0); c += __uint_as_float(p1);
        }
        a += __shfl_xor(a, 32); c += __shfl_xor(c, 32);
        const float mean = a * (1.0f / D);
        const float var = fmaxf(c * (1.0f / D) - mean * mean, 0.f);
        if (lane < 32) {
          const u32x4 g = {__float_as_uint(-mean), tA, __float_as_uint(var + 1e-5f), tB};
          const u32 so = OFF_S2 + (tbs * 32 + lane) * 16; STC4(g, so, rs, 0);
        }
      }
      if (hf == 0) TS(5); else TS(7);
    }
    TSOUT();

    return;
  }

  const int etb = tid >> 3;
  const u32 go = etb * 57344 + jc * 256 + tq * 16;
  u32x4 gu0, gu1, gx0, gx1, gs0, gs1;
#pragma unroll
  for (int i = NE; i < 14; ++i) DMAA(i);
  LDC4(gu0, go, rs, OFF_U, 0); LDC4(gu1, go, rs, OFF_U, 128);
  LDC4(gx0, go, rs, OFF_X, 0); LDC4(gx1, go, rs, OFF_X, 128);
  const u32 so_ = tid * 16;
  LDC4(gs0, so_, rs, OFF_ST, 0); LDC4(gs1, so_, rs, OFF_ST + 8192, 0);
  {
    const int c = tid >> 8, t8 = tid & 255;
    const u32 d0 = kin[0], d1 = kin[1], d2 = kin[2], d3 = kin[3], d4 = t8 < 255 ? knx[0] : 0u, d5 = t8 < 255 ? knx[1] : 0u;
    const u32 e0_ = __builtin_amdgcn_alignbit(d1, d0, 16), e1 = __builtin_amdgcn_alignbit(d2, d1, 16), e2 = __builtin_amdgcn_alignbit(d3, d2, 16),
              e3 = __builtin_amdgcn_alignbit(d4, d3, 16), e4 = __builtin_amdgcn_alignbit(d5, d4, 16);
    LAS char* p = lds + L_F + c * 4 * KC + (t8 + 2) * 16;
    *(LAS u32x4*)(p) = u32x4{d0, d1, d2, d3};
    *(LAS u32x4*)(p + KC) = u32x4{e0_, e1, e2, e3};
    *(LAS u32x4*)(p + 2 * KC) = u32x4{d1, d2, d3, d4};
    *(LAS u32x4*)(p + 3 * KC) = u32x4{e1, e2, e3, e4};
    if (t8 == 0) {
      LAS char* z = lds + L_F + c * 4 * KC;
#pragma unroll
      for (int s = 0; s < 4; ++s) *(LAS u32x4*)(z + s * KC) = u32x4{0u, 0u, 0u, 0u};
      *(LAS u32x4*)(z + 16) = u32x4{0u, 0u, 0u, 0u};
      *(LAS u32x4*)(z + 16 + KC) = u32x4{0u, 0u, 0u, d0 << 16};
      *(LAS u32x4*)(z + 16 + 2 * KC) = u32x4{0u, 0u, 0u, d0};
      *(LAS u32x4*)(z + 16 + 3 * KC) = u32x4{0u, 0u, d0 << 16, e0_};
    }
    if (t8 >= 64 && t8 < 94) *(LAS u32x4*)(lds + L_U + c * US + (t8 - 64) * 16) = u32x4{0u, 0u, 0u, 0u};
  }
  TS(4);
  WAITV(6);
  FPFLAG();
  bar();
  TS(5);
  u32x4 W[28];
  const LAS char* wb = lds + (4 * (lane >> 5) + ((lane >> 4) & 1)) * 1024 + ((lane & 15) >> 2) * 256 +
                       ((w ^ (4 * ((lane >> 4) & 1) + ((lane & 15) >> 2))) * 32) + (lane & 3) * 8;
#pragma unroll
  for (int i = 0; i < 14; ++i) W[i] = trd2(wb + L_WA + i * 8192, 2048);
  WAITV(5); TIE(gu0); TIE(gu1);
  if (!allz(gbad(gu0, tA, tB) || gbad(gu1, tA, tB))) {

#pragma unroll 1
    for (int it = 0; it < MAXSPIN; ++it) {
      LDC4(gu0, go, rs, OFF_U, 0); LDC4(gu1, go, rs, OFF_U, 128); WAITV(0); TIE(gu0); TIE(gu1);
      if (allz(gbad(gu0, tA, tB) || gbad(gu1, tA, tB))) break;
      __builtin_amdgcn_s_sleep(1);
    }

  }
  {
    LAS char* up = lds + L_U + (30 + etb * 4 + (tq >> 1)) * 16 + ((tq & 1) ^ 1) * 8;
    *(LAS u32x2*)up = u32x2{gu0[0], gu0[2]};
    *(LAS u32x2*)(up + US) = u32x2{gu1[0], gu1[2]};
  }
  TS(6);
  const u32 f1o = OFF_FP + (etb * 32 + (jc >> 3)) * 8;
  const int l14 = lane < 14 ? lane : 13, bj = 8 * (l14 < 7 ? l14 : l14 - 7) + w;
  const u32 f3o = OFF_FP + ((l14 < 7 ? 2 * P : 32 + 2 * P) * 32 + (bj < 28 ? bj : bj + 4)) * 8;
  u32x2 f1, f3;
  const u32 ao = OFF_A + (P * 448 + G * 8 + w) * 1024 + lane * 16;
  float cs0 = 0.f, cs1 = 0.f;
#pragma unroll
  for (int i = 0; i < 14; ++i) {
    const u32 a0 = W[i][0], a1 = W[i][1], a2 = W[i][2], a3 = W[i][3];
    cs0 = dot2(a0, 0x3f803f80u, cs0); cs1 = dot2(a1, 0x3f803f80u, cs1); cs0 = dot2(a2, 0x3f803f80u, cs0); cs1 = dot2(a3, 0x3f803f80u, cs1);
  }
  bar();
  TS(7);
  float xp[8], gg[8];
  u32x4 xs[7];
  const u32 xo = OFF_H + (2 * P * 28 + w) * 1024 + lane * 16;
  const u32 ho = OFF_H + etb * 28672 + jc * 128 + (tq & 1) * 64 + (tq >> 1) * 16;
  const LAS char* pr = lds + (w & 3) * 512 + (2 * (lane >> 3) + (tq >> 2)) * 32 + (((tq & 3) ^ ((lane >> 4) & 2)) << 3);
#define CONV_EPI(base) do { \
    float y[8]; \
    _Pragma("unroll") for (int i = 0; i < 8; ++i) y[i] = 0.f; \
    _Pragma("unroll") for (int c = 0; c < 2; ++c) \
    _Pragma("unroll") for (int i = 0; i < 4; ++i) { \
      const u32x2 p_ = *(const LAS u32x2*)(pr + (base) + (c * 4 + i) * 2048); const u32 p0 = p_[0], p1 = p_[1]; \
      y[4 * c] += bf_lo(p0); y[4 * c + 1] += bf_hi(p0); y[4 * c + 2] += bf_lo(p1); y[4 * c + 3] += bf_hi(p1); } \
    const u32 a0 = pk2(xp[0] + gg[0] * y[0], xp[1] + gg[1] * y[1]), a1 = pk2(xp[2] + gg[2] * y[2], xp[3] + gg[3] * y[3]); \
    const u32 b0 = pk2(xp[4] + gg[4] * y[4], xp[5] + gg[5] * y[5]), b1 = pk2(xp[6] + gg[6] * y[6], xp[7] + gg[7] * y[7]); \
    const bool od = tq & 1; \
    const u32 r0 = __builtin_amdgcn_mov_dpp(od ? a0 : b0, 0xb1, 0xf, 0xf, true), r1 = __builtin_amdgcn_mov_dpp(od ? a1 : b1, 0xb1, 0xf, 0xf, true); \
    const u32x4 o = {nop32(od ? r0 : a0), nop32(od ? r1 : a1), nop32(od ? b0 : r0), nop32(od ? b1 : r1)}; \
    flagwait(f1, f1o, rs, t0, t1); \
    STC4(o, ho, rs, 0); } while (0)
  {
    const int cc = w >> 2, p = (w >> 1) & 1, qq = w & 1, r = lane & 15, g = lane >> 4;
    const int s = (r + 1) & 3;
    const LAS char* ab = lds + L_F + (cc * 4 + s) * KC + 2 * (32 * p + 25 + r - 8 * g - s) + qq * 128;
    const LAS char* bb = lds + L_U + cc * US + (4 - 4 * p + 2 * r + g) * 16 + (1 - qq) * 128;
    LAS char* pb = lds + w * 2048 + 32 * r + 8 * (g ^ ((r >> 2) & 2));
    u32x4 A[24], B[16];
    f32x4 acc[8];
#define LDA(i, m) do { const u32x2 lo_ = *(const volatile LAS u32x2*)(ab + (m) * 256), hi_ = *(const volatile LAS u32x2*)(ab + (m) * 256 + 8); \
                       A[i] = u32x4{lo_[0], lo_[1], hi_[0], hi_[1]}; } while (0)
#define LDBU(j) B[j] = *(const LAS u32x4*)(bb + (j) * 256)
#define PUT(base, t) *(LAS u32x2*)(pb + (base) + 512 * ((t) & 3)) = u32x2{pk2(acc[t][0], acc[t][1]), pk2(acc[t][2], acc[t][3])}
#define DMAB(i) DMA(L_WB + (i) * 8192 + w * 1024, wvo, rw, wso + (14 + (i)) * 16 * (F * 2))
#define DMAB4(i) DMA(L_WB + (i) * 8192 + w * 1024 - 4096, wvo, rw, wso + ((14 + (i)) * 16 - 8) * (F * 2))
#define PZ(t) do { const u32x4 ones = {~0u, ~0u, ~0u, ~0u}; const u32 a_ = ao + (t) * 112 * 1024; STC4(ones, a_, rs, 0); } while (0)
    LDA(0, 0); LDBU(1); LDBU(3); LDBU(5); LDBU(7); LDA(1, 1); LDBU(0); LDBU(2); LDBU(4); LDBU(6); LDA(2, 2);
#pragma unroll
    for (int t = 0; t < 8; ++t) acc[t] = f32x4{0.f, 0.f, 0.f, 0.f};
    SB();
    SFOR(i, 24)
      constexpr int m = i < 8 ? i : i - 8;
      if constexpr (i + 3 < 24) LDA(i + 3, i + 3 < 8 ? i + 3 : i + 3 - 8);
      if constexpr (i < 4) { LDBU((i < 2 ? 9 : 8) + 4 * (i & 1)); LDBU((i < 2 ? 11 : 10) + 4 * (i & 1)); }
      if constexpr (i < 8) {
#pragma unroll
        for (int t = m / 2; t < 4; ++t) acc[t] = mfma16(A[i], B[2 * t - m + 1], acc[t]);
      } else {
#pragma unroll
        for (int t = (m / 2 > 4 ? m / 2 : 4); t < 8; ++t) acc[t] = mfma16(A[i], B[2 * t - m + 1], acc[t]);
      }
      if constexpr (i == 2 || i == 4 || i == 6 || i == 8) PUT(L_PE, i / 2 - 1);
      if constexpr (i == 18 || i == 20 || i == 22) PUT(L_PL, i / 2 - 5);
      if constexpr (i == 0 || i == 2 || i == 5 || i == 7) DMAB(i == 0 ? 0 : i == 2 ? 1 : i == 5 ? 2 : 3);
      if constexpr (i < 3) PZ(i);
      if constexpr (i == 3) {
        WAITV(6); TIE(gx0); TIE(gx1); TIE(gs0); TIE(gs1);
        if (!allz(gbad(gx0, tA, tB) || gbad(gx1, tA, tB) || gbad(gs0, tA, tB) || gbad(gs1, tA, tB))) {

#pragma unroll 1
          for (int it = 0; it < MAXSPIN; ++it) {
            LDC4(gx0, go, rs, OFF_X, 0); LDC4(gx1, go, rs, OFF_X, 128); LDC4(gs0, so_, rs, OFF_ST, 0); LDC4(gs1, so_, rs, OFF_ST + 8192, 0);
            WAITV(0); TIE(gx0); TIE(gx1); TIE(gs0); TIE(gs1);
            if (allz(gbad(gx0, tA, tB) || gbad(gx1, tA, tB) || gbad(gs0, tA, tB) || gbad(gs1, tA, tB))) break;
            __builtin_amdgcn_s_sleep(1);
          }

        }
        PZ(3);
        LDC2(f1, f1o, rs, 0); LDC2(f3, f3o, rs, 0);
        TS(8);
      }
      if constexpr (i >= 4 && i < 8) {
        const int j = i - 4;
        const u32 sw = j == 0 ? gs0[0] : j == 1 ? gs0[2] : j == 2 ? gs1[0] : gs1[2];
        const float mean = bf_lo(sw), rsd = __builtin_amdgcn_rcpf(bf_hi(sw));
        const u32 x0 = gx0[j & 2], x1 = gx1[j & 2], v0 = gu0[2 - (j & 2)], v1 = gu1[2 - (j & 2)];
        const float xa = (j & 1) ? bf_hi(x0) : bf_lo(x0), xb_ = (j & 1) ? bf_hi(x1) : bf_lo(x1);
        const float ua = (j & 1) ? bf_lo(v0) : bf_hi(v0), ub = (j & 1) ? bf_lo(v1) : bf_hi(v1);
        gg[j] = (xa - mean) * rsd; gg[4 + j] = (xb_ - mean) * rsd;
        xp[j] = xa + gg[j] * (ua * bf_lo(dd)); xp[4 + j] = xb_ + gg[4 + j] * (ub * bf_hi(dd));
      }
      if constexpr (i == 8) bar();
      if constexpr (i == 9) {
        TS(9);
        WAITV(2); TIE(f1); TIE(f3);
        if (w < 4) CONV_EPI(L_PE);
        TS(10);
      }
      if constexpr (i >= 9 && i < 15) { if (w >= 4) { DMAB(i - 5); DMAB4(i - 5); } }
      SB();
    SEND
    PUT(L_PL, 7);
  }
  TS(11);
  flagwait(f3, f3o, rs, t0, t1);
  LDC4(xs[0], xo, rs, 0, 0); LDC4(xs[1], xo, rs, 8192, 0); LDC4(xs[2], xo, rs, 16384, 0); LDC4(xs[3], xo, rs, 24576, 0);
  WAITV(4);
  { const u32x2 f = {t0, t1}; const u32 fo = OFF_FA + (P * 112 + G * 8 + w) * 8; STF(f, fo, rs); }
  bar();
  TS(12);
#pragma unroll
  for (int i = 0; i < 4; ++i) DMA(L_WC + i * 8192 + w * 1024, wvo, rw, wso + (24 + i) * 16 * (F * 2));
  LDC4(xs[4], xo, rs, 32768, 0); LDC4(xs[5], xo, rs, 40960, 0); LDC4(xs[6], xo, rs, 49152, 0);
  if (w >= 4) CONV_EPI(L_PL);
#pragma unroll
  for (int i = 0; i < 10; ++i) W[14 + i] = trd2(wb + L_WB + i * 8192, 2048);
#pragma unroll
  for (int i = 14; i < 24; ++i) {
    const u32 a0 = W[i][0], a1 = W[i][1], a2 = W[i][2], a3 = W[i][3];
    cs0 = dot2(a0, 0x3f803f80u, cs0); cs1 = dot2(a1, 0x3f803f80u, cs1); cs0 = dot2(a2, 0x3f803f80u, cs0); cs1 = dot2(a3, 0x3f803f80u, cs1);
  }

#define XCHECK(i0, n, so0) do { u32 m_ = 0; \
    _Pragma("unroll") for (int i_ = 0; i_ < n; ++i_) { TIE(xs[i0 + i_]); m_ = umax3(m_, xs[i0 + i_][0], xs[i0 + i_][1]); m_ = umax3(m_, xs[i0 + i_][2], xs[i0 + i_][3]); } \
    if (!allz(m_ == ~0u)) {  \
      _Pragma("unroll 1") for (int it = 0; it < MAXSPIN; ++it) { \
        _Pragma("unroll") for (int i_ = 0; i_ < n; ++i_) LDC4(xs[i0 + i_], xo, rs, (so0) + (i0 + i_) * 8192, 0); \
        WAITV(0); m_ = 0; \
        _Pragma("unroll") for (int i_ = 0; i_ < n; ++i_) { TIE(xs[i0 + i_]); m_ = umax3(m_, xs[i0 + i_][0], xs[i0 + i_][1]); m_ = umax3(m_, xs[i0 + i_][2], xs[i0 + i_][3]); } \
        if (allz(m_ == ~0u)) break; \
        __builtin_amdgcn_s_sleep(1); } \
       } } while (0)
  TS(13);
  if (w < 4) WAITV(8); else WAITV(9);
  XCHECK(0, 4, 0);
  LAS char* xw = lds + w * 1024 + lane * 16;
#pragma unroll
  for (int i = 0; i < 3; ++i) *(LAS u32x4*)(xw + L_X0 + i * 8192) = xs[i];
  if (w < 4) *(LAS u32x4*)(xw + L_X0 + 24576) = xs[3];
  bar();
  TS(14);
  const LAS char* xb = lds + (8 * (lane >> 5) + ((lane & 15) >> 2)) * 64 + ((lane >> 4) & 1) * 32 + (lane & 3) * 8;
  const u32 s2o = OFF_S2 + (2 * P * 32 + (lane & 31)) * 16;
  const int hf = xcd & 1, g7 = bi >> 2;
  const int t2 = (w >> 1) ^ 2, kh = w & 1;
  const u32 dvo = (lane >> 3) * (D * 2) + (((lane & 7) ^ ((lane >> 2) & 4)) << 4) + g7 * 128;
  const u32 dso = (896 * hf + 16 * w) * (D * 2);
#define DMA2(p) DMA(((p) >> 1) * 16384 + (2 * w + ((p) & 1)) * 1024, dvo, r2, dso + (((p) >> 1) * 128 + ((p) & 1) * 8) * (D * 2))
  const int l28 = lane < 28 ? lane : 27;
  const u32 fao = OFF_FA + (P * 112 + hf * 56 + kh * 28 + l28) * 8;
  const u32 bo = OFF_A + ((P * 4 + t2) * 112 + hf * 56 + kh * 28) * 1024 + lane * 16;
#define LDQ(n) LDC4(Bq[n], bo, rs, ((n) >> 2) * 4096, ((n) & 3) * 1024)
#define AST(t) do { const u32x4 o_ = {nop32(o4[0]), nop32(o4[1]), nop32(o4[2]), nop32(o4[3])}; const u32 a_ = ao + (t) * 112 * 1024; STC4(o_, a_, rs, 0); } while (0)
  u32x4 Bf[4][28], Bq[28], st, st3, res, c4a, c4b;
  u32x2 fa;
  f32x16 acc[2];
  float nm, karg, cv, hv0, ga, up, ee;
  u32 o4[4];
#define XT(t) ((t) == 0 ? L_X0 : (t) == 1 ? L_X1 : (t) == 2 ? L_X2 : L_X3)
#define LDB(t, ks) Bf[t][ks] = trd2(xb + XT(t) + (ks) * 1024, 256)
#define S2OFF(t) ((t) == 0 ? 0 : (t) == 1 ? 512 : (t) == 2 ? 16384 : 16896)
#define S2CHECK(st, t) do { TIE(st); \
    if (!allz(gbad(st, tA, tB))) {  \
      _Pragma("unroll 1") for (int it = 0; it < MAXSPIN; ++it) { \
        LDC4(st, s2o, rs, S2OFF(t), 0); WAITV(0); TIE(st); \
        if (allz(gbad(st, tA, tB))) break; \
        __builtin_amdgcn_s_sleep(1); } \
       } \
    { const u32 a_ = st[0], b_ = st[2]; nm = __uint_as_float(a_); cv = __uint_as_float(b_); karg = -1.44269504088896340736f * __builtin_amdgcn_rsqf(cv); } } while (0)
#define CSLD(h) do { c4a = *(const LAS u32x4*)(lds + L_CS + w * 128 + (lane >> 5) * 16 + (h) * 32); \
                     c4b = *(const LAS u32x4*)(lds + L_CS + w * 128 + (lane >> 5) * 16 + (h) * 32 + 64); } while (0)
#define EPI_A(a, i) do { const u32 ca_ = c4a[(i) & 3], cb_ = c4b[(i) & 3]; ga = a[i] + nm * __uint_as_float(ca_); up = a[(i) + 8] + nm * __uint_as_float(cb_); \
                         ee = __builtin_amdgcn_exp2f(ga * karg); } while (0)
#define EPI_B(i) do { const float h_ = (ga * up) * __builtin_amdgcn_rcpf(ee * cv + cv); if ((i) & 1) o4[(i) >> 1] = pk2(hv0, h_); else hv0 = h_; } while (0)
  LDB(0, 0); LDB(0, 1); LDB(0, 2);
  SB();
  SFOR(t, 4)
#pragma unroll
    for (int i = 0; i < 16; ++i) acc[t & 1][i] = 0.f;
    SFOR(ks, 28)
      constexpr int bs = t == 0 ? 22 : 24;
      if constexpr (ks + 3 < 28) LDB(t, ks + 3);
      if constexpr (ks == bs && t < 3) bar();
      if constexpr (ks + 3 >= 28 && t < 3) LDB(t + 1, ks + 3 - 28);
      if constexpr (t == 0 && (ks == 22 || ks == 23)) { W[2 * ks - 20] = trd2(wb + L_WC + (2 * ks - 44) * 8192, 2048); W[2 * ks - 19] = trd2(wb + L_WC + (2 * ks - 43) * 8192, 2048); }
      acc[t & 1] = mfma32(W[ks], Bf[t][ks], acc[t & 1]);
      if constexpr (ks == 4) if (t == t2) res = trd2(xb + XT(t) + (4 * g7 + 2 * hf + kh) * 1024 - (lane >> 5) * 256, 512);
      if constexpr (t == 0) {
        if constexpr (ks == 1) if (w >= 4) *(LAS u32x4*)(xw + L_X1 - 4096) = xs[3];
        if constexpr (ks == 18) { if (w < 4) WAITV(0); else WAITV(1); XCHECK(4, 3, 0); }
        if constexpr (ks >= 19 && ks <= 21) *(LAS u32x4*)(xw + L_X1 + 4096 + (ks - 19) * 8192) = xs[ks - 15];
        if constexpr (ks == 23) LDC4(st, s2o, rs, S2OFF(0), 0);
        if constexpr (ks >= 24) LDC4(xs[ks - 24], xo, rs, 32 * 28672 + (ks - 24) * 8192, 0);
        if constexpr (ks >= 24) {
          const u32 a0 = W[ks][0], a1 = W[ks][1], a2 = W[ks][2], a3 = W[ks][3];
          cs0 = dot2(a0, 0x3f803f80u, cs0); cs1 = dot2(a1, 0x3f803f80u, cs1); cs0 = dot2(a2, 0x3f803f80u, cs0); cs1 = dot2(a3, 0x3f803f80u, cs1);
        }
      } else {
        if constexpr (t == 1 && ks < 3) LDC4(xs[4 + ks], xo, rs, 32 * 28672 + (4 + ks) * 8192, 0);
        if constexpr (t == 1 && ks == 3) { float c = cs0 + cs1; c += __shfl_xor(c, 32); if (lane < 32) *(LAS float*)(lds + L_CS + w * 128 + lane * 4) = c; }
        if constexpr (ks == 8) CSLD(0);
        if constexpr (ks == 9) { if constexpr (t == 1) WAITV(7); else if constexpr (t == 2) WAITV(11); else WAITV(5); S2CHECK(st, t - 1); }
        if constexpr (ks >= 10 && ks < 26) { if constexpr (ks & 1) EPI_B((ks - 10) >> 1); else EPI_A(acc[(t - 1) & 1], (ks - 10) >> 1); }
        if constexpr (ks == 16) CSLD(1);
        if constexpr (ks == 26) AST(t - 1);
        if constexpr (t == 1) {
          if constexpr (ks == 17) { WAITV(3); XCHECK(0, 4, 32 * 28672); }
          if constexpr (ks >= 18 && ks <= 21) *(LAS u32x4*)(xw + L_X2 + (ks - 18) * 8192) = xs[ks - 18];
          if constexpr (ks == 23) LDC4(st, s2o, rs, S2OFF(1), 0);
          if constexpr (ks == 25 || ks == 27) DMA2((ks - 25) >> 1);
        }
        if constexpr (t == 2) {
          if constexpr (ks < 8) DMA2(ks + 2);
          if constexpr (ks == 11) XCHECK(4, 3, 32 * 28672);
          if constexpr (ks == 12 || ks == 14 || ks == 18) *(LAS u32x4*)(xw + L_X2 + (ks == 12 ? 4 : ks == 14 ? 5 : 6) * 8192) = xs[ks == 12 ? 4 : ks == 14 ? 5 : 6];
          if constexpr (ks == 20) LDC2(fa, fao, rs, 0);
          if constexpr (ks == 22) LDC4(st, s2o, rs, S2OFF(2), 0);
          if constexpr (ks == 23) LDC4(st3, s2o, rs, S2OFF(3), 0);
          if constexpr (ks == 25 || ks == 27) DMA2(10 + ((ks - 25) >> 1));
          if constexpr (ks == 25) *(LAS u32*)(lds + L_EF + w * 4) = 0u;
        }
        if constexpr (t == 3) {
          if constexpr (ks < 2) DMA2(12 + ks);
          if constexpr (ks == 11) { TIE(fa); flagwait(fa, fao, rs, t0, t1); }
          if constexpr (ks == 13) { WAITV(0); bar(); }
          if constexpr (ks >= 14) { if (w >= 4) { LDQ(2 * ks - 28); LDQ(2 * ks - 27); } }
        }
      }
      SB();
    SEND
    TS(15 + t);
  SEND
  CSLD(0);
  S2CHECK(st3, 3);
#pragma unroll
  for (int i = 0; i < 8; ++i) { if (i == 4) CSLD(1); EPI_A(acc[1], i); EPI_B(i); }
  AST(3);
  TS(19);

  {
    if (w < 4) {
      const u32 po = bo - lane * 16 + (lane < 56 ? lane >> 1 : 27) * 1024 + (lane & 1) * 1020;
      u32 pv;
#pragma unroll 1
      for (int it = 0; it < MAXSPIN; ++it) {
        LDC1(pv, po, rs); WAITV(0); TIE(pv);
        if (allz(pv == ~0u)) break;
      }
      TS(20);
#pragma unroll
      for (int n = 0; n < 28; ++n) LDQ(n);
    }
    TS(21);
    u32x4 Af[56];
    const LAS char* vb = lds + kh * 57344 + (4 * (lane >> 5) + ((lane & 15) >> 2)) * 128 + ((hf ^ ((lane >> 3) & 1)) * 64) + ((((lane >> 4) ^ kh) & 1) * 32) + (lane & 3) * 8;
    const LAS char* vc = lds + (((u32)(size_t)vb - (u32)(size_t)lds) ^ 64u);
#define LDV(n) do { Af[2 * (n)] = trd2(vb + (n) * 2048, 1024); Af[2 * (n) + 1] = trd2(vc + (n) * 2048, 1024); } while (0)
    LDV(0); LDV(1);
#pragma unroll
    for (int i = 0; i < 16; ++i) { acc[0][i] = 0.f; acc[1][i] = 0.f; }
    SB();
    SFOR(n, 28)
      if constexpr ((n & 3) == 0) {
        WAITV(24 - n);
        u32 m_ = 0;
#pragma unroll
        for (int i = 0; i < 4; ++i) { TIE(Bq[n + i]); m_ = umax3(m_, Bq[n + i][0], Bq[n + i][1]); m_ = umax3(m_, Bq[n + i][2], Bq[n + i][3]); }
        if (!allz(m_ == ~0u)) {
#pragma unroll 1
          for (int it = 0; it < MAXSPIN; ++it) {
            LDQ(n); LDQ(n + 1); LDQ(n + 2); LDQ(n + 3);
            WAITV(0); m_ = 0;
#pragma unroll
            for (int i = 0; i < 4; ++i) { TIE(Bq[n + i]); m_ = umax3(m_, Bq[n + i][0], Bq[n + i][1]); m_ = umax3(m_, Bq[n + i][2], Bq[n + i][3]); }
            if (allz(m_ == ~0u)) break;
            __builtin_amdgcn_s_sleep(1);
          }
        }
        if constexpr (n == 0) TS(22);
      }
      if constexpr (n + 2 < 28) LDV(n + 2);
      acc[0] = mfma32(Af[2 * n], Bq[n], acc[0]);
      acc[1] = mfma32(Af[2 * n + 1], Bq[n], acc[1]);
      SB();
    SEND
    TS(23);
    LAS char* ew = lds + (w < 6 ? L_EA + w * 2048 : L_EB + (w - 6) * 2048) + lane * 16;
    const LAS char* er = lds + (w < 6 ? L_EA + (w ^ 1) * 2048 : L_EB + ((w ^ 1) - 6) * 2048) + lane * 16;
    *(LAS u32x4*)(ew + 1024) = u32x4{pk2(acc[1][8], acc[1][9]), pk2(acc[1][10], acc[1][11]), pk2(acc[1][12], acc[1][13]), pk2(acc[1][14], acc[1][15])};
    *(LAS u32x4*)ew = u32x4{pk2(acc[0][8], acc[0][9]), pk2(acc[0][10], acc[0][11]), pk2(acc[0][12], acc[0][13]), pk2(acc[0][14], acc[0][15])};
    asm volatile("s_waitcnt lgkmcnt(0)" ::: "memory");
    *(volatile LAS u32*)(lds + L_EF + w * 4) = 1u;
#pragma unroll 1
    for (int it = 0; it < MAXSPIN; ++it) {
      const u32 f = *(const volatile LAS u32*)(lds + L_EF + (w ^ 1) * 4);
      if (__builtin_amdgcn_readfirstlane(f)) break;
    }
    asm volatile("" ::: "memory");
    const u32x4 p1 = *(const LAS u32x4*)(er + 1024), p0 = *(const LAS u32x4*)er;
    const u32 qo = OFF_Q + ((P * 14 + g7) * 8 + w) * 2048 + lane * 16;
    {
      const u32 a0 = p1[0], a1 = p1[1], a2 = p1[2], a3 = p1[3];
      const u32x4 g0 = {pk2(acc[1][0] + bf_lo(a0), acc[1][1] + bf_hi(a0)), tA, pk2(acc[1][2] + bf_lo(a1), acc[1][3] + bf_hi(a1)), tB};
      const u32x4 g1 = {pk2(acc[1][4] + bf_lo(a2), acc[1][5] + bf_hi(a2)), tA, pk2(acc[1][6] + bf_lo(a3), acc[1][7] + bf_hi(a3)), tB};
      const u32 a_ = qo + (hf ^ 1) * (7 * 8 * 2048);
      STC4(g0, a_, rs, 0); STC4(g1, a_, rs, 1024);
    }
    TS(24);
    float y[8];
#pragma unroll
    for (int e = 0; e < 4; ++e) {
      const u32 r = res[e], p = p0[e];
      y[2 * e] = acc[0][2 * e] + bf_lo(p) + bf_lo(r); y[2 * e + 1] = acc[0][2 * e + 1] + bf_hi(p) + bf_hi(r);
    }
    u32x4 g0, g1;
    const u32 ro = qo + hf * (7 * 8 * 2048);
#pragma unroll 1
    for (int it = 0; it < MAXSPIN; ++it) {
      LDC4(g0, ro, rs, 0, 0); LDC4(g1, ro, rs, 0, 1024); WAITV(0); TIE(g0); TIE(g1);
      if (allz(gbad(g0, tA, tB) || gbad(g1, tA, tB))) break;
    }
    TS(25);
    const u32 r0 = g0[0], r1 = g0[2], r2_ = g1[0], r3 = g1[2];
    const int tok = (t2 < 2 ? 2 * P + t2 : 30 + 2 * P + t2) * 32 + (lane & 31);
    u16* op = out + (size_t)tok * D + 64 * g7 + 32 * hf + 16 * kh + 4 * (lane >> 5);
    *(u32x2*)op = u32x2{pk2(y[0] + bf_lo(r0), y[1] + bf_hi(r0)), pk2(y[2] + bf_lo(r1), y[3] + bf_hi(r1))};
    *(u32x2*)(op + 8) = u32x2{pk2(y[4] + bf_lo(r2_), y[5] + bf_hi(r2_)), pk2(y[6] + bf_lo(r3), y[7] + bf_hi(r3))};
  }
  if (b == 0 && w == 0) { const u32x2 f = {t0, t1}; STF(f, zero, rs); }
  TSOUT();
}

extern "C" void kernel_launch(void* const* d_in, const int* in_sizes, int n_in, void* d_out, int out_size,
                              void* d_ws, size_t ws_size, hipStream_t stream) {
  (void)hipFuncSetAttribute((const void*)fused, hipFuncAttributeMaxDynamicSharedMemorySize, L_SIZE);
  fused<<<256, 512, L_SIZE, stream>>>((const u16*)d_in[0], (const u16*)d_in[1], (const u16*)d_in[2], (const u16*)d_in[3],
                                      (const u16*)d_in[4], (const u16*)d_in[5], (const u16*)d_in[7], (const u16*)d_in[8],
                                      (const u16*)d_in[9], (u16*)d_out, (char*)d_ws);
}
```

```cpp
#include <hip/hip_runtime.h>
#include <hip/hip_bf16.h>
#include <stdint.h>

typedef unsigned short u16;
typedef unsigned int u32;
typedef unsigned long long u64;
typedef u32 u32x2 __attribute__((ext_vector_type(2)));
typedef u32 u32x4 __attribute__((ext_vector_type(4)));
typedef short s16x4 __attribute__((ext_vector_type(4)));
typedef float f32x4 __attribute__((ext_vector_type(4)));
typedef float f32x16 __attribute__((ext_vector_type(16)));
typedef __bf16 bf16x8 __attribute__((ext_vector_type(8)));
typedef __bf16 bf16x2 __attribute__((ext_vector_type(2)));
#define DI __device__ __forceinline__
#define LAS __attribute__((address_space(3)))
#define SB() __builtin_amdgcn_sched_barrier(0)
template <int V> struct IC { static constexpr int v = V; };
template <int I, int N, class Fn> DI void sfor(Fn&& f) { if constexpr (I < N) { f(IC<I>{}); sfor<I + 1, N>(f); } }
#define SFOR(var, n) sfor<0, n>([&](auto ic_##var) __attribute__((always_inline)) { constexpr int var = decltype(ic_##var)::v;
#define SEND });

constexpr int S = 2048, D = 448, F = 1792;
constexpr u32 OFF_E = 0;
constexpr u32 OFF_FP = 4096;
constexpr u32 OFF_FA = OFF_FP + 16384;
constexpr u32 OFF_ST = OFF_FA + 16384;
constexpr u32 OFF_S2 = OFF_ST + 16384;
constexpr u32 SZ_SD = S * D * 2;
constexpr u32 OFF_U = OFF_S2 + 32768;
constexpr u32 OFF_X = OFF_U + 2 * SZ_SD;
constexpr u32 OFF_H = OFF_X + 2 * SZ_SD;
constexpr u32 OFF_A = OFF_H + SZ_SD;
constexpr u32 OFF_Q = OFF_A + S * F * 2;
constexpr u32 WS_END = OFF_Q + 16 * 14 * 4 * 4096;
constexpr int KC = 4160;
constexpr int L_F = 0;
constexpr int L_U = 8 * KC;
constexpr int US = 286 * 16;
constexpr int L_PE = 42496;
constexpr int L_G0 = 58880, L_G1 = 108032;
constexpr int L_GZ = 81920;
constexpr int L_XB = 102400;
constexpr int L_XA = 143360;
constexpr int L_PV = 9216, L_PQ = L_U;
constexpr int L_EX = 114688, L_EF = 159744;
constexpr int L_CS = 162816;
constexpr int L_SIZE = 163840;
constexpr int MAXSPIN = 1 << 18;
#define NE0 0
#define NE1 3
#define ROTA 2
#define ROTB 7
constexpr int wslot(int i) { return i < 6 ? L_G0 + i * 8192 : i < 8 ? L_PE + (i - 6) * 8192 : i < 14 ? L_G1 + (i - 8) * 8192
                                  : i < 20 ? L_G0 + (i - 14) * 8192 : i < 26 ? L_G1 + (i - 20) * 8192 : L_GZ + (i - 26) * 8192; }

#define TSDECL
#define TS(i)
#define TSOUT()

DI float bf_lo(u32 v) { return __uint_as_float(v << 16); }
DI float bf_hi(u32 v) { return __uint_as_float(v & 0xffff0000u); }
DI u32 pk2(float lo, float hi) { bf16x2 r; r[0] = (__bf16)lo; r[1] = (__bf16)hi; return __builtin_bit_cast(u32, r); }
DI float rbf(float x) { return (float)(__bf16)x; }
DI u32 plo(u32 a, u32 b) { return __builtin_amdgcn_perm(b, a, 0x05040100u); }
DI u32 phi(u32 a, u32 b) { return __builtin_amdgcn_perm(b, a, 0x07060302u); }
DI f32x16 mfma32(u32x4 a, u32x4 b, f32x16 c) {
  return __builtin_amdgcn_mfma_f32_32x32x16_bf16(__builtin_bit_cast(bf16x8, a), __builtin_bit_cast(bf16x8, b), c, 0, 0, 0);
}
DI f32x4 mfma16(u32x4 a, u32x4 b, f32x4 c) {
  return __builtin_amdgcn_mfma_f32_16x16x32_bf16(__builtin_bit_cast(bf16x8, a), __builtin_bit_cast(bf16x8, b), c, 0, 0, 0);
}
DI float dot2(u32 a, u32 b, float c) { return __builtin_amdgcn_fdot2_f32_bf16(__builtin_bit_cast(bf16x2, a), __builtin_bit_cast(bf16x2, b), c, false); }
DI u32x2 trd(const LAS char* p) { return __builtin_bit_cast(u32x2, __builtin_amdgcn_ds_read_tr16_b64_v4i16((LAS s16x4*)p)); }
DI u32x4 trd2(const LAS char* p, int d) { const u32x2 a = trd(p), b = trd(p + d); return u32x4{a[0], a[1], b[0], b[1]}; }
DI u32 umax3(u32 a, u32 b, u32 c) { const u32 m = a > b ? a : b; return m > c ? m : c; }
DI u32 nop32(u32 x) { return x < 0xfffffffeu ? x : 0xfffffffeu; }
DI bool allz(bool bad) { return __builtin_amdgcn_ballot_w64(bad) == 0; }
#define RARE(bad) __builtin_expect(!allz(bad), 0)
DI u32x4 mksrd(const void* p, u32 bytes) { const u64 a = (u64)p; return u32x4{(u32)a, (u32)(a >> 32) & 0xffffu, bytes, 0x00020000u}; }

#define LD1(d, vo, srd, off) asm volatile("buffer_load_dword %0, %1, %2, 0 offen offset:%3" : "=v"(d) : "v"(vo), "s"(srd), "i"(off) : "memory")
#define LD2(d, vo, srd, off) asm volatile("buffer_load_dwordx2 %0, %1, %2, 0 offen offset:%3" : "=v"(d) : "v"(vo), "s"(srd), "i"(off) : "memory")
#define LD4(d, vo, srd, off) asm volatile("buffer_load_dwordx4 %0, %1, %2, 0 offen offset:%3" : "=v"(d) : "v"(vo), "s"(srd), "i"(off) : "memory")
#define LDC1(d, vo, srd) asm volatile("buffer_load_dword %0, %1, %2, 0 offen sc1" : "=v"(d) : "v"(vo), "s"(srd) : "memory")
#define LDC2(d, vo, srd, so) asm volatile("buffer_load_dwordx2 %0, %1, %2, %3 offen sc1" : "=v"(d) : "v"(vo), "s"(srd), "s"(so) : "memory")
#define LDC4(d, vo, srd, so, off) asm volatile("buffer_load_dwordx4 %0, %1, %2, %3 offen offset:%4 sc1" : "=v"(d) : "v"(vo), "s"(srd), "s"(so), "i"(off) : "memory")
#define STC4(v, vo, srd, off) asm volatile("buffer_store_dwordx4 %0, %1, %2, 0 offen offset:%3 sc1\n\ts_nop 1" :: "v"(v), "v"(vo), "s"(srd), "i"(off) : "memory")
#define STF(v, vo, srd) asm volatile("s_mov_b64 exec, 1\n\tbuffer_store_dwordx2 %0, %1, %2, 0 offen sc1\n\ts_mov_b64 exec, -1" :: "v"(v), "v"(vo), "s"(srd) : "memory")
#define DMA(la, vo, srd, so) asm volatile("s_mov_b32 m0, %0\n\ts_nop 0\n\tbuffer_load_dwordx4 %1, %2, %3 offen lds" :: "s"(la), "v"(vo), "s"(srd), "s"(so) : "memory")
#define WAITV(n) asm volatile("s_waitcnt vmcnt(%0)" :: "i"(n) : "memory")
#define TIE(x) asm volatile("" : "+v"(x))
DI void bar() { asm volatile("s_waitcnt lgkmcnt(0)\n\ts_barrier" ::: "memory"); }
DI bool gbad(u32x4 g, u32 tA, u32 tB) { return g[1] != tA || g[3] != tB; }
DI void flagwait(u32x2 f, u32 vo, u32x4 srd, u32 t0, u32 t1) {
  if (RARE(f[0] != t0 || f[1] != t1)) {
#pragma unroll 1
    for (int it = 0; it < MAXSPIN; ++it) {
      __builtin_amdgcn_s_sleep(1);
      LDC2(f, vo, srd, 0); WAITV(0); TIE(f);
      if (allz(f[0] != t0 || f[1] != t1)) break;
    }

  }
}

DI LAS char* lp(u32 a) { return (LAS char*)(size_t)a; }
DI u32 opq(u32 a) { TIE(a); return a; }
#define RAWBAR() asm volatile("s_barrier" ::: "memory")

__global__ __launch_bounds__(512, 2) void fused(const u16* __restrict__ q, const u16* __restrict__ k, const u16* __restrict__ v,
                                                const u16* __restrict__ shk, const u16* __restrict__ ssk, const u16* __restrict__ ssd,
                                                const u16* __restrict__ w1, const u16* __restrict__ w3, const u16* __restrict__ w2,
                                                u16* __restrict__ out, char* ws) {
  extern __shared__ __attribute__((aligned(16))) char lds_[];
  const int tid = threadIdx.x, lane = tid & 63;
  const int w = __builtin_amdgcn_readfirstlane(tid >> 6);
  const int b = blockIdx.x, xcd = b & 7, bi = b >> 3;
  const bool spare = bi >= 28;
  TSDECL; TS(0);
  const u32x4 rs = mksrd(ws, WS_END), rq = mksrd(q, SZ_SD), rk = mksrd(k, SZ_SD), rv = mksrd(v, SZ_SD), rh = mksrd(shk, D * 8),
              rf = mksrd(ssk, SZ_SD), rw = mksrd((w & 1) ? w3 : w1, D * F * 2), r2 = mksrd(w2, D * F * 2);
  const int jc = spare ? 0 : xcd * 28 + bi;
  const int P = (xcd >> 1) * 4 + (bi & 3), G = (xcd & 1) * 7 + (spare ? bi - 28 : bi >> 2);
  const int rot = (ROTA * (bi & 3) + ROTB * (xcd >> 1) + (spare ? 14 : 0)) % 28;

  const int tb = xcd * 8 + (bi >> 2), cq = bi & 3;
  const int tq = lane & 7;
  u32x4 kin; u32x2 knx;
  const u32 zero = 0;
  u32 ov;
  {
    const int c = (lane & 15) ^ w;
    const u32 o = (tb * 32 + 4 * w + (lane >> 4)) * (D * 2) + cq * 224 + c * 16;
    ov = c < 14 ? o : 0x80000000u;
    const u32 ok = (c < 14 && (tb | w)) ? o - 4 * (D * 2) : 0x80000000u;
    DMA(w * 1024, ok, rk, 0); DMA(L_PV + w * 1024, ov, rv, 0);
  }
  const u32 wvo = (lane >> 4) * (F * 2) + G * 256 + (((lane & 15) ^ ((4 * (w & 1) + (lane >> 4)) << 1)) << 4);
  const u32 wso = (w >> 1) * 4 * (F * 2) + rot * 16 * (F * 2);
  const u32 fvo = jc * (2 * S * 2) + tid * 16;
#define WSO(i) (wso + (i) * 16 * (F * 2) - ((i) + rot >= 28 ? 28 * 16 * (F * 2) : 0))
#define DMAW(i) DMA(wslot(i) + w * 1024, wvo, rw, WSO(i))
#define DMAW4(i) DMA(wslot(i) + w * 1024 - 4096, wvo, rw, WSO(i) - 8 * (F * 2))
#define MKTAG() do { const u64 tg = (((u64)(u32)__builtin_amdgcn_readfirstlane(e0[1]) << 32) | (u32)__builtin_amdgcn_readfirstlane(e0[0])) + 1; \
    t0 = (u32)tg; t1 = (u32)(tg >> 32); tA = t0; tB = t0 ^ t1 ^ 0x9E3779B9u; } while (0)
  u32 t0, t1, tA, tB;
  if (w < 7) {
    const int cp = 8 * w + (lane >> 3);
    u32x4 sk; u32x2 e0;
    LDC2(e0, zero, rs, 0);
    { const u32 so = (cq * 112 + 2 * cp) * 8; LD4(sk, so, rh, 0); }
    DMA(L_PQ + w * 1024, ov, rq, 0);
    RAWBAR();
    { const u32x4 ones = {~0u, ~0u, ~0u, ~0u}; const u32 po = OFF_H + tb * 28672 + cq * 7168 + tid * 16; STC4(ones, po, rs, 0); }
    LD4(kin, fvo, rf, 0); LD2(knx, fvo, rf, 16);
#pragma unroll
    for (int i = 0; i < NE0; ++i) DMAW(i);
    WAITV(3 + NE0);
    TIE(e0); TIE(sk);
    TS(1);
    RAWBAR();
    const int c = 2 * w + (lane >> 5);
    const LAS char* pa = lp(tq * 1024 + ((c ^ tq) << 4) + ((lane >> 3) & 3) * 4);
    const LAS char* pb = lp(tq * 1024 + ((c ^ ((tq + 1) & 7)) << 4) + ((lane >> 3) & 3) * 4);
    u32 kk[7], vv[4], qq[4];
#pragma unroll
    for (int j = 0; j < 7; ++j) kk[j] = *(const LAS u32*)((j < 3 ? pa : pb) + (j + 1) * 256);
#pragma unroll
    for (int j = 0; j < 4; ++j) vv[j] = *(const LAS u32*)(pa + L_PV + j * 256);
#pragma unroll
    for (int j = 0; j < 4; ++j) qq[j] = *(const LAS u32*)(pa + L_PQ + j * 256);
    MKTAG();
    const u32 s0 = sk[0], s1 = sk[1], s2 = sk[2], s3 = sk[3];
    const float a0 = bf_lo(s0), a1 = bf_hi(s0), a2 = bf_lo(s1), a3 = bf_hi(s1);
    const float c0 = bf_lo(s2), c1 = bf_hi(s2), c2 = bf_lo(s3), c3 = bf_hi(s3);
    float u0[4], u1[4];
#pragma unroll
    for (int j = 0; j < 4; ++j) {
      u0[j] = (bf_lo(kk[j]) * a3 + bf_lo(kk[j + 1]) * a2 + bf_lo(kk[j + 2]) * a1 + bf_lo(kk[j + 3]) * a0) * bf_lo(vv[j]);
      u1[j] = (bf_hi(kk[j]) * c3 + bf_hi(kk[j + 1]) * c2 + bf_hi(kk[j + 2]) * c1 + bf_hi(kk[j + 3]) * c0) * bf_hi(vv[j]);
    }
    const u32 uo = OFF_U + tb * 57344 + (cq * 112 + 2 * cp) * 128 + tq * 16;
    { const u32x4 g = {pk2(u0[3], u0[2]), tA, pk2(u0[1], u0[0]), tB}; STC4(g, uo, rs, 0); }
    { const u32x4 g = {pk2(u1[3], u1[2]), tA, pk2(u1[1], u1[0]), tB}; STC4(g, uo, rs, 128); }
    const u32 xo = uo + (OFF_X - OFF_U);
    { const u32x4 g = {plo(qq[0], qq[1]), tA, plo(qq[2], qq[3]), tB}; STC4(g, xo, rs, 0); }
    { const u32x4 g = {phi(qq[0], qq[1]), tA, phi(qq[2], qq[3]), tB}; STC4(g, xo, rs, 128); }
    WAITV(2 + NE0 + 4);
    { const u32x2 f = {t0, t1}; const u32 fpo = OFF_FP + (tb * 32 + cq * 7 + w) * 8; STF(f, fpo, rs); }
  } else {
    u32x4 sq[7]; u32x2 e0;
    LDC2(e0, zero, rs, 0);
    { const u32 o = (tb * 32 + 28 + (lane >> 4)) * (D * 2) + cq * 224 + (lane & 15) * 16; const u32 ok = (lane & 15) < 14 ? o : 0x80000000u; DMA(8192, ok, rk, 0); }
    DMA(L_PQ + w * 1024, ov, rq, 0);
    const u32 vo = (tb * 32 + 8 * cq + (lane >> 3)) * (D * 2) + (lane & 7) * 16;
    LD4(sq[0], vo, rq, 0); LD4(sq[1], vo, rq, 128); LD4(sq[2], vo, rq, 256); LD4(sq[3], vo, rq, 384);
    LD4(sq[4], vo, rq, 512); LD4(sq[5], vo, rq, 640); LD4(sq[6], vo, rq, 768);
    RAWBAR();
    LD4(kin, fvo, rf, 0); LD2(knx, fvo, rf, 16);
#pragma unroll
    for (int i = 0; i < NE0; ++i) DMAW(i);
    WAITV(9 + NE0);
    TIE(e0);
    TS(1);
    RAWBAR();
    MKTAG();
    WAITV(2 + NE0);
#pragma unroll
    for (int c = 0; c < 7; ++c) TIE(sq[c]);
    float s = 0.f, ss = 0.f, s_ = 0.f, ss_ = 0.f;
#pragma unroll
    for (int c = 0; c < 7; ++c) {
      const u32 x0 = sq[c][0], x1 = sq[c][1], x2 = sq[c][2], x3 = sq[c][3];
      s = dot2(x0, 0x3f803f80u, s); ss = dot2(x0, x0, ss); s_ = dot2(x1, 0x3f803f80u, s_); ss_ = dot2(x1, x1, ss_);
      s = dot2(x2, 0x3f803f80u, s); ss = dot2(x2, x2, ss); s_ = dot2(x3, 0x3f803f80u, s_); ss_ = dot2(x3, x3, ss_);
    }
    s += s_; ss += ss_;
#define DPPADD(x, ctl) x += __uint_as_float(__builtin_amdgcn_mov_dpp(__float_as_uint(x), ctl, 0xf, 0xf, true))
    DPPADD(s, 0xb1); DPPADD(ss, 0xb1); DPPADD(s, 0x4e); DPPADD(ss, 0x4e); DPPADD(s, 0x141); DPPADD(ss, 0x141);
    const float mean = s * (1.0f / D);
    const float var = rbf(fmaxf(ss * (1.0f / D) - mean * mean, 0.f));
    const u32 st = pk2(mean, sqrtf(rbf(var + 1e-5f)));
    const u32 sb = __builtin_amdgcn_mov_dpp(st, 0x108, 0xf, 0xf, true);
    if ((lane & 15) == 0) { const u32x4 g = {st, tA, sb, tB}; const u32 so = OFF_ST + ((lane >> 4) & 1) * 8192 + (tb * 8 + 2 * cq + (lane >> 5)) * 16; STC4(g, so, rs, 0); }
  }
  TS(2);

  if (spare) {
    WAITV(0); SB();
    const int lane = opq(tid & 63);
    const int s = xcd * 4 + (bi - 28);
    const int b3 = w < 4 ? w + 24 : w + 16;
    LAS char* blk = lp(65536 + w * 4096);
    const LAS char* xb = blk + (8 * (lane >> 5) + ((lane & 15) >> 2)) * 64 + ((lane >> 4) & 1) * 32 + (lane & 3) * 8;
    bar();
#pragma unroll 1
    for (int hf = 0; hf < 2; ++hf) {
      const int tbs = s + 32 * hf;
      {
        const u32 fo = OFF_FP + (tbs * 32 + ((lane & 3) == 3 ? b3 : w + 8 * (lane & 3))) * 8;
        u32x2 f; LDC2(f, fo, rs, 0); WAITV(0); TIE(f);
        flagwait(f, fo, rs, t0, t1);
      }
      u32x4 xs[4];
      const u32 xo = OFF_H + (tbs * 28 + w) * 1024 + lane * 16, xo3 = OFF_H + (tbs * 28 + b3) * 1024 + lane * 16;
#pragma unroll 1
      for (int it = 0; it < MAXSPIN; ++it) {
        LDC4(xs[0], xo, rs, 0, 0); LDC4(xs[1], xo, rs, 8192, 0); LDC4(xs[2], xo, rs, 16384, 0); LDC4(xs[3], xo3, rs, 0, 0);
        WAITV(0);
        u32 m = 0;
#pragma unroll
        for (int i = 0; i < 4; ++i) { TIE(xs[i]); m = umax3(m, xs[i][0], xs[i][1]); m = umax3(m, xs[i][2], xs[i][3]); }
        if (allz(m == ~0u)) break;
      }
      if (hf == 0) TS(4); else TS(6);
#pragma unroll
      for (int i = 0; i < 4; ++i) *(LAS u32x4*)(blk + i * 1024 + lane * 16) = xs[i];
      float sm = 0.f, ss = 0.f, sm_ = 0.f, ss_ = 0.f;
#pragma unroll
      for (int i = 0; i < 4; ++i) {
        u32x4 x = trd2(xb + i * 1024, 256);
        if (i == 3 && w >= 4) x = u32x4{0u, 0u, 0u, 0u};
        const u32 x0 = x[0], x1 = x[1], x2 = x[2], x3 = x[3];
        sm = dot2(x0, 0x3f803f80u, sm); ss = dot2(x0, x0, ss); sm_ = dot2(x1, 0x3f803f80u, sm_); ss_ = dot2(x1, x1, ss_);
        sm = dot2(x2, 0x3f803f80u, sm); ss = dot2(x2, x2, ss); sm_ = dot2(x3, 0x3f803f80u, sm_); ss_ = dot2(x3, x3, ss_);
      }
      *(LAS u32x2*)lp(32768 + hf * 4096 + (lane * 8 + w) * 8) = u32x2{__float_as_uint(sm + sm_), __float_as_uint(ss + ss_)};
      bar();
      if (w == 0) {
        float a = 0.f, c = 0.f;
#pragma unroll
        for (int i = 0; i < 4; ++i) {
          const u32x4 p = *(const LAS u32x4*)lp(32768 + hf * 4096 + lane * 64 + i * 16);
          const u32 p0 = p[0], p1 = p[1], p2 = p[2], p3 = p[3];
          a += __uint_as_float(p0) + __uint_as_float(p2); c += __uint_as_float(p1) + __uint_as_float(p3);
        }
        a += __shfl_xor(a, 32); c += __shfl_xor(c, 32);
        const float mean = a * (1.0f / D);
        const float var = fmaxf(c * (1.0f / D) - mean * mean, 0.f);
        if (lane < 32) {
          const u32x4 g = {__float_as_uint(-mean), tA, __float_as_uint(var + 1e-5f), tB};
          const u32 so = OFF_S2 + (tbs * 32 + lane) * 16; STC4(g, so, rs, 0);
        }
      }
      if (hf == 0) TS(5); else TS(7);
    }
    TSOUT();
    return;
  }

#define XI 22
#define DI0 10
  const u32 dd = *(const u32*)(ssd + 2 * jc);
  const int etb = tid >> 3;
  const u32 go = etb * 57344 + jc * 256 + tq * 16, so_ = tid * 16;
  u32x4 gu0, gu1, gx0, gx1, gs0, gs1;
  u32x2 f1, f3;
#pragma unroll
  for (int i = NE0; i < 14; ++i) DMAW(i);
#define LDG() do { LDC4(gu0, go, rs, OFF_U, 0); LDC4(gu1, go, rs, OFF_U, 128); LDC4(gx0, go, rs, OFF_X, 0); LDC4(gx1, go, rs, OFF_X, 128); \
                   LDC4(gs0, so_, rs, OFF_ST, 0); LDC4(gs1, so_, rs, OFF_ST + 8192, 0); } while (0)
#define GBAD() (gbad(gu0, tA, tB) || gbad(gu1, tA, tB) || gbad(gx0, tA, tB) || gbad(gx1, tA, tB) || gbad(gs0, tA, tB) || gbad(gs1, tA, tB))
#define TIEG() do { TIE(gu0); TIE(gu1); TIE(gx0); TIE(gx1); TIE(gs0); TIE(gs1); } while (0)
  LDG();
  const u32 f1o = OFF_FP + (etb * 32 + (jc >> 3)) * 8;
  u32 f3o;
  {
    const int l14 = lane < 14 ? lane : 13, bj = 8 * (l14 < 7 ? l14 : l14 - 7) + w, tl = bj >= 28, kk = bj - 28 * tl + rot;
    f3o = OFF_FP + (((l14 < 7 ? 2 * P : 32 + 2 * P) + tl) * 32 + (kk >= 28 ? kk - 28 : kk)) * 8;
  }
  LDC2(f1, f1o, rs, 0); LDC2(f3, f3o, rs, 0);
  TS(3);
  WAITV(14); TIE(kin); TIE(knx);
  bar();
  TS(4);
  u32x4 W[28];
  const u32 wb = (4 * (lane >> 5) + ((lane >> 4) & 1)) * 1024 + ((lane & 15) >> 2) * 256 +
                 ((w ^ (4 * ((lane >> 4) & 1) + ((lane & 15) >> 2))) * 32) + (lane & 3) * 8;
  const LAS char* wbA = lp(opq(wb + L_PE));
  const LAS char* wbB = lp(opq(wb + L_G1));
#define LDW(i) W[i] = wslot(i) >= L_G1 ? trd2(wbB + (wslot(i) - L_G1), 2048) : trd2(wbA + (wslot(i) - L_PE), 2048)
  const u32 sel1 = (lane & 15) == ((lane >> 4) & 1) ? 0x3f803f80u : 0u;
  const u32x4 sel = {sel1, sel1, sel1, sel1};
  f32x4 cs[2] = {{0.f, 0.f, 0.f, 0.f}, {0.f, 0.f, 0.f, 0.f}};
#define CSUM(i) cs[(i) & 1] = mfma16(W[i], sel, cs[(i) & 1])
#pragma unroll
  for (int i = 0; i < 8; ++i) LDW(i);
  {
    const int c = tid >> 8, t8 = tid & 255;
    const u32 d0 = kin[0], d1 = kin[1], d2 = kin[2], d3 = kin[3], d4 = t8 < 255 ? knx[0] : 0u, d5 = t8 < 255 ? knx[1] : 0u;
    const u32 e0_ = __builtin_amdgcn_alignbit(d1, d0, 16), e1 = __builtin_amdgcn_alignbit(d2, d1, 16), e2 = __builtin_amdgcn_alignbit(d3, d2, 16),
              e3 = __builtin_amdgcn_alignbit(d4, d3, 16), e4 = __builtin_amdgcn_alignbit(d5, d4, 16);
    LAS char* p = lp(L_F + c * 4 * KC + (t8 + 2) * 16);
    *(LAS u32x4*)(p) = u32x4{d0, d1, d2, d3};
    *(LAS u32x4*)(p + KC) = u32x4{e0_, e1, e2, e3};
    *(LAS u32x4*)(p + 2 * KC) = u32x4{d1, d2, d3, d4};
    *(LAS u32x4*)(p + 3 * KC) = u32x4{e1, e2, e3, e4};
    if (t8 == 0) {
      LAS char* z = lp(L_F + c * 4 * KC);
#pragma unroll
      for (int s = 0; s < 4; ++s) *(LAS u32x4*)(z + s * KC) = u32x4{0u, 0u, 0u, 0u};
      *(LAS u32x4*)(z + 16) = u32x4{0u, 0u, 0u, 0u};
      *(LAS u32x4*)(z + 16 + KC) = u32x4{0u, 0u, 0u, d0 << 16};
      *(LAS u32x4*)(z + 16 + 2 * KC) = u32x4{0u, 0u, 0u, d0};
      *(LAS u32x4*)(z + 16 + 3 * KC) = u32x4{0u, 0u, d0 << 16, e0_};
    }
    if (t8 >= 64 && t8 < 94) *(LAS u32x4*)lp(L_U + c * US + (t8 - 64) * 16) = u32x4{0u, 0u, 0u, 0u};
  }
#pragma unroll
  for (int i = 0; i < 8; ++i) CSUM(i);
  TS(5);
  WAITV(2); TIEG();
  if (RARE(GBAD())) {
#pragma unroll 1
    for (int it = 0; it < MAXSPIN; ++it) {
      LDG(); WAITV(0); TIEG();
      if (allz(GBAD())) break;
    }
  }
  TS(6);
  {
    LAS char* up = lp(L_U + (30 + etb * 4 + (tq >> 1)) * 16 + ((tq & 1) ^ 1) * 8);
    *(LAS u32x2*)up = u32x2{gu0[0], gu0[2]};
    *(LAS u32x2*)(up + US) = u32x2{gu1[0], gu1[2]};
  }
  bar();
  TS(7);
  const u32 ao = OFF_A + (P * 448 + G * 8 + w) * 1024 + lane * 16;
  float xp[8], gg[8];
  u32x4 xs[7];
  u32 xso[7];
#pragma unroll
  for (int i = 0; i < 7; ++i) { const int s = w + 8 * i, tl = s >= 28, kk = s - 28 * tl + rot; xso[i] = tl * 28672 + (kk >= 28 ? kk - 28 : kk) * 1024; }
  const u32 xo = OFF_H + 2 * P * 28672 + lane * 16, xo2 = xo + 32 * 28672;
#define LDX(i, vo) LDC4(xs[i], vo, rs, xso[i], 0)
  const u32 ho = OFF_H + etb * 28672 + jc * 128 + (tq & 1) * 64 + (tq >> 1) * 16;
  const LAS char* pr = lp(L_PE + (w & 3) * 512 + (2 * (lane >> 3) + (tq >> 2)) * 32 + (((tq & 3) ^ ((lane >> 4) & 2)) << 3));
#define CONV_EPI() do { \
    float y[8]; \
    _Pragma("unroll") for (int i = 0; i < 8; ++i) y[i] = 0.f; \
    _Pragma("unroll") for (int c = 0; c < 2; ++c) \
    _Pragma("unroll") for (int i = 0; i < 4; ++i) { \
      const u32x2 p_ = *(const LAS u32x2*)(pr + (c * 4 + i) * 2048); const u32 p0 = p_[0], p1 = p_[1]; \
      y[4 * c] += bf_lo(p0); y[4 * c + 1] += bf_hi(p0); y[4 * c + 2] += bf_lo(p1); y[4 * c + 3] += bf_hi(p1); } \
    const u32 a0 = pk2(xp[0] + gg[0] * y[0], xp[1] + gg[1] * y[1]), a1 = pk2(xp[2] + gg[2] * y[2], xp[3] + gg[3] * y[3]); \
    const u32 b0 = pk2(xp[4] + gg[4] * y[4], xp[5] + gg[5] * y[5]), b1 = pk2(xp[6] + gg[6] * y[6], xp[7] + gg[7] * y[7]); \
    const bool od = tq & 1; \
    const u32 r0 = __builtin_amdgcn_mov_dpp(od ? a0 : b0, 0xb1, 0xf, 0xf, true), r1 = __builtin_amdgcn_mov_dpp(od ? a1 : b1, 0xb1, 0xf, 0xf, true); \
    const u32x4 o = {nop32(od ? r0 : a0), nop32(od ? r1 : a1), nop32(od ? b0 : r0), nop32(od ? b1 : r1)}; \
    STC4(o, ho, rs, 0); } while (0)
  {
    const int cc = w >> 2, p = (w >> 1) & 1, qq = w & 1, r = lane & 15, g = lane >> 4;
    const int s = (r + 1) & 3;
    const LAS char* ab = lp(L_F + (cc * 4 + s) * KC + 2 * (32 * p + 25 + r - 8 * g - s) + qq * 128);
    const LAS char* bb = lp(L_U + cc * US + (4 - 4 * p + 2 * r + g) * 16 + (1 - qq) * 128);
    LAS char* pb = lp(L_PE + w * 2048 + 32 * r + 8 * (g ^ ((r >> 2) & 2)));
    u32x4 A[24], B[16];
    f32x4 acc[8];
#define LDA(i, m) do { const u32x2 lo_ = *(const volatile LAS u32x2*)(ab + (m) * 256), hi_ = *(const volatile LAS u32x2*)(ab + (m) * 256 + 8); \
                       A[i] = u32x4{lo_[0], lo_[1], hi_[0], hi_[1]}; } while (0)
#define LDBU(j) B[j] = *(const LAS u32x4*)(bb + (j) * 256)
#define PUT(t) *(LAS u32x2*)(pb + 512 * ((t) & 3)) = u32x2{pk2(acc[t][0], acc[t][1]), pk2(acc[t][2], acc[t][3])}
    LDA(0, 0); LDBU(1); LDBU(3); LDBU(5); LDBU(7); LDA(1, 1); LDBU(0); LDBU(2); LDBU(4); LDBU(6); LDA(2, 2);
#pragma unroll
    for (int t = 0; t < 8; ++t) acc[t] = f32x4{0.f, 0.f, 0.f, 0.f};
    SB();
    SFOR(i, 24)
      constexpr int m = i < 8 ? i : i - 8;
      if constexpr (i + 3 < 24) LDA(i + 3, i + 3 < 8 ? i + 3 : i + 3 - 8);
      if constexpr (i >= 1 && i < 7) LDW(7 + i);
      if constexpr (i >= 2 && i < 6) { LDBU((i < 4 ? 9 : 8) + 4 * (i & 1)); LDBU((i < 4 ? 11 : 10) + 4 * (i & 1)); }
      if constexpr (i < 8) {
#pragma unroll
        for (int t = m / 2; t < 4; ++t) acc[t] = mfma16(A[i], B[2 * t - m + 1], acc[t]);
      } else {
#pragma unroll
        for (int t = (m / 2 > 4 ? m / 2 : 4); t < 8; ++t) acc[t] = mfma16(A[i], B[2 * t - m + 1], acc[t]);
      }
      if constexpr (i >= 18 && i < 24) CSUM(i - 10);
      if constexpr (i == 2 || i == 4 || i == 6 || i == 8) PUT(i / 2 - 1);
      if constexpr (i == 18 || i == 20 || i == 22) PUT(i / 2 - 5);
      if constexpr (i < 6) DMAW(14 + i);
      if constexpr (i >= 4 && i < 8) {
        const int j = i - 4;
        const u32 sw = j == 0 ? gs0[0] : j == 1 ? gs0[2] : j == 2 ? gs1[0] : gs1[2];
        const float mean = bf_lo(sw), rsd = __builtin_amdgcn_rcpf(bf_hi(sw));
        const u32 x0 = gx0[j & 2], x1 = gx1[j & 2], v0 = gu0[2 - (j & 2)], v1 = gu1[2 - (j & 2)];
        const float xa = (j & 1) ? bf_hi(x0) : bf_lo(x0), xb_ = (j & 1) ? bf_hi(x1) : bf_lo(x1);
        const float ua = (j & 1) ? bf_lo(v0) : bf_hi(v0), ub = (j & 1) ? bf_lo(v1) : bf_hi(v1);
        gg[j] = (xa - mean) * rsd; gg[4 + j] = (xb_ - mean) * rsd;
        xp[j] = xa + gg[j] * (ua * bf_lo(dd)); xp[4 + j] = xb_ + gg[4 + j] * (ub * bf_hi(dd));
      }
      if constexpr (i == 7) { WAITV(6); TIE(f1); TIE(f3); flagwait(f1, f1o, rs, t0, t1); flagwait(f3, f3o, rs, t0, t1); }
      if constexpr (i == 8) { bar(); TS(8); }
      if constexpr (i == 9) { if (w < 4) CONV_EPI(); TS(9); }
      if constexpr (i >= DI0 && i < DI0 + 6) { if (w >= 4) { DMAW(i - DI0 + 20); DMAW4(i - DI0 + 20); } }
      if constexpr (i == 17) { RAWBAR(); TS(10); }
      if constexpr (i == XI) { LDX(0, xo); LDX(1, xo); LDX(2, xo); LDX(3, xo); }
      SB();
    SEND
    PUT(7);
  }
  if constexpr (XI >= 24) { LDX(0, xo); LDX(1, xo); LDX(2, xo); LDX(3, xo); }
  TS(11);
  if (w < 4) WAITV(5); else WAITV(4);
  bar();
  TS(12);
  LDX(4, xo); LDX(5, xo); LDX(6, xo);
  if (w >= 4) CONV_EPI();
#pragma unroll
  for (int i = 14; i < 20; ++i) LDW(i);
#pragma unroll
  for (int i = 14; i < 20; ++i) CSUM(i);

#define XCHECK(i0, n, vo) do { u32 m_ = 0; \
    _Pragma("unroll") for (int i_ = 0; i_ < n; ++i_) { TIE(xs[i0 + i_]); m_ = umax3(m_, xs[i0 + i_][0], xs[i0 + i_][1]); m_ = umax3(m_, xs[i0 + i_][2], xs[i0 + i_][3]); } \
    if (RARE(m_ == ~0u)) {  \
      _Pragma("unroll 1") for (int it = 0; it < MAXSPIN; ++it) { \
        _Pragma("unroll") for (int i_ = 0; i_ < n; ++i_) LDX(i0 + i_, vo); \
        WAITV(0); m_ = 0; \
        _Pragma("unroll") for (int i_ = 0; i_ < n; ++i_) { TIE(xs[i0 + i_]); m_ = umax3(m_, xs[i0 + i_][0], xs[i0 + i_][1]); m_ = umax3(m_, xs[i0 + i_][2], xs[i0 + i_][3]); } \
        if (allz(m_ == ~0u)) break; } \
       } } while (0)
  TS(13);
  if (w < 4) WAITV(3); else WAITV(4);
  XCHECK(0, 4, xo);
  LAS char* xw = lp(w * 1024 + lane * 16);
  LAS char* xwB = lp(opq(L_XB + w * 1024 + lane * 16));
#pragma unroll
  for (int i = 0; i < 4; ++i) *(LAS u32x4*)(xw + i * 8192) = xs[i];
  bar();
  TS(14);
  const u32 xb_ = (8 * (lane >> 5) + ((lane & 15) >> 2)) * 64 + ((lane >> 4) & 1) * 32 + (lane & 3) * 8;
  const LAS char* xbA = lp(opq(xb_));
  const LAS char* xbB = lp(opq(xb_ + L_XB));
  const u32 s2o = OFF_S2 + (2 * P * 32 + (lane & 31)) * 16;
  const int hf = xcd & 1, g7 = bi >> 2;
  const int t2 = w >> 1, kh = w & 1;
  const u32 dvo = (lane >> 3) * (D * 2) + (((lane & 7) ^ ((lane >> 2) & 4)) << 4) + g7 * 128;
  const u32 dso = (896 * hf + 8 * w) * (D * 2);
#define DMA2(p) DMA((p) * 8192 + w * 1024, dvo, r2, dso + (((p) % 7) * 128 + ((p) / 7) * 64) * (D * 2))
  const int l28 = lane < 28 ? lane : 27;
  const u32 fao = OFF_FA + (P * 112 + hf * 56 + 4 * kh + 8 * (l28 >> 2) + (l28 & 3)) * 8;
  const u32 bo = OFF_A + ((P * 4 + t2) * 112 + hf * 56 + 4 * kh) * 1024 + lane * 16;
  u32 rso;
  { const int kk = 4 * g7 + 2 * hf + kh - rot, s = (t2 & 1) * 28 + (kk < 0 ? kk + 28 : kk);
    rso = xb_ - (lane >> 5) * 256 + (t2 < 2 ? s * 1024 : s < 16 ? L_XA + s * 1024 : L_XB + (s - 16) * 1024); }
#define LDQ(n) asm volatile("buffer_load_dwordx4 %0, %1, %2, %3 offen offset:%4 sc1" : "+v"(Bq[n]) : "v"(bo), "s"(rs), "s"(((n) >> 2) * 8192), "i"(((n) & 3) * 1024) : "memory")
#define LDQC(con, n, c) asm volatile("s_cmp_eq_u32 %5, %6\n\ts_cbranch_scc0 .Lq%=\n\tbuffer_load_dwordx4 %0, %1, %2, %3 offen offset:%4 sc1\n.Lq%=:" \
    : con(Bq[n]) : "v"(bo), "s"(rs), "s"(((n) >> 2) * 8192), "i"(((n) & 3) * 1024), "s"(t2), "i"(c) : "memory", "scc")
#define LDQ4(n, c) asm volatile("s_cmp_eq_u32 %7, %8\n\ts_cbranch_scc0 .Lq%=\n\tbuffer_load_dwordx4 %0, %4, %5, %6 offen sc1\n\tbuffer_load_dwordx4 %1, %4, %5, %6 offen offset:1024 sc1\n\t" \
    "buffer_load_dwordx4 %2, %4, %5, %6 offen offset:2048 sc1\n\tbuffer_load_dwordx4 %3, %4, %5, %6 offen offset:3072 sc1\n.Lq%=:" \
    : "+v"(Bq[n]), "+v"(Bq[(n) + 1]), "+v"(Bq[(n) + 2]), "+v"(Bq[(n) + 3]) : "v"(bo), "s"(rs), "s"(((n) >> 2) * 8192), "s"(t2), "i"(c) : "memory", "scc")
#define PZ(t) do { const u32x4 ones = {~0u, ~0u, ~0u, ~0u}; const u32 a_ = ao + (t) * 112 * 1024; STC4(ones, a_, rs, 0); } while (0)
#define AST(t) do { const u32x4 o_ = {nop32(o4[0]), nop32(o4[1]), nop32(o4[2]), nop32(o4[3])}; const u32 a_ = ao + (t) * 112 * 1024; STC4(o_, a_, rs, 0); } while (0)
  u32x4 Bf[4][28], Bq[28], st0, st1, res, c4a, c4b;
  u32x2 fa;
  f32x16 acc[2];
  float nm, karg, cv, hv0, ga, up, ee;
  u32 o4[4];
#define XSL(t, ks) (((t) & 1) * 28 + (ks))
#define LDB(t, ks) Bf[t][ks] = (t) < 2 ? trd2(xbA + XSL(t, ks) * 1024, 256) : trd2(xbB + (XSL(t, ks) < 16 ? L_XA - L_XB + XSL(t, ks) * 1024 : (XSL(t, ks) - 16) * 1024), 256)
#define XW2(i) *(LAS u32x4*)(xwB + ((i) < 2 ? L_XA - L_XB + (i) * 8192 : ((i) - 2) * 8192)) = xs[i]
#define S2OFF(t) ((t) == 0 ? 0 : (t) == 1 ? 512 : (t) == 2 ? 16384 : 16896)
#define S2CHECK(st, t) do { TIE(st); \
    if (RARE(gbad(st, tA, tB))) {  \
      _Pragma("unroll 1") for (int it = 0; it < MAXSPIN; ++it) { \
        LDC4(st, s2o, rs, S2OFF(t), 0); WAITV(0); TIE(st); \
        if (allz(gbad(st, tA, tB))) break; } \
       } \
    { const u32 a_ = st[0], b_ = st[2]; nm = __uint_as_float(a_); cv = __uint_as_float(b_); karg = -1.44269504088896340736f * __builtin_amdgcn_rsqf(cv); } } while (0)
#define CSLD(h) do { c4a = *(const LAS u32x4*)lp(L_CS + w * 128 + (lane >> 5) * 16 + (h) * 32); \
                     c4b = *(const LAS u32x4*)lp(L_CS + w * 128 + (lane >> 5) * 16 + (h) * 32 + 64); } while (0)
#define EPI_A(a, i) do { const u32 ca_ = c4a[(i) & 3], cb_ = c4b[(i) & 3]; TIE(nm); ga = a[i] + nm * __uint_as_float(ca_); TIE(ga); up = a[(i) + 8] + nm * __uint_as_float(cb_); \
                         ee = __builtin_amdgcn_exp2f(ga * karg); TIE(ee); TIE(up); } while (0)
#define EPI_B(i) do { TIE(ee); float h_ = (ga * up) * __builtin_amdgcn_rcpf(ee * cv + cv); TIE(h_); if ((i) & 1) { o4[(i) >> 1] = pk2(hv0, h_); TIE(o4[(i) >> 1]); } else hv0 = h_; } while (0)
  LDB(0, 0); LDB(0, 1); LDB(0, 2);
  SB();
  SFOR(t, 4)
#pragma unroll
    for (int i = 0; i < 16; ++i) acc[t & 1][i] = 0.f;
    SFOR(ks, 28)
      constexpr int e0s = t == 1 ? 10 : 2;
      if constexpr (ks + 3 < 28) LDB(t, ks + 3);
      if constexpr (ks == 24 && (t == 1 || t == 2)) bar();
      if constexpr (ks + 3 >= 28 && t < 3) LDB(t + 1, ks + 3 - 28);
      if constexpr (t == 0 && ks < 6) LDW(20 + ks);
      if constexpr (t == 0 && ks == 19) { LDW(26); LDW(27); }
      acc[t & 1] = mfma32(W[ks], Bf[t][ks], acc[t & 1]);
      if constexpr (ks == 4) if (t == t2) res = trd2(lp(rso), 512);
      if constexpr (t == 0) {
        if constexpr (ks < 2) DMAW(26 + ks);
        if constexpr (ks >= 2 && ks < 6) PZ(ks - 2);
        if constexpr (ks >= 6 && ks < 9) DMA2(ks + 1);
        if constexpr (ks >= 4 && ks < 10) CSUM(16 + ks);
        if constexpr (ks == 12) { WAITV(9); XCHECK(4, 3, xo); }
        if constexpr (ks >= 13 && ks < 16) *(LAS u32x4*)(xw + (ks - 9) * 8192) = xs[ks - 9];
        if constexpr (ks == 18) { WAITV(7); bar(); }
        if constexpr (ks == 20) { WAITV(3); const u32x2 f = {t0, t1}; const u32 fo = OFF_FA + (P * 112 + G * 8 + w) * 8; STF(f, fo, rs); }
        if constexpr (ks == 22 || ks == 23) CSUM(ks + 4);
        if constexpr (ks == 26) { const f32x4 c = cs[0] + cs[1]; if ((lane & 15) < 2) *(LAS f32x4*)lp(L_CS + w * 128 + (lane & 1) * 64 + (lane >> 4) * 16) = c; }
      } else {
        if constexpr (ks == e0s - 2) CSLD(0);
        if constexpr (ks == 27 && t < 3) CSLD(0);
        if constexpr (ks == e0s - 1) { if constexpr (t == 1) { WAITV(7); S2CHECK(st0, 0); } else if constexpr (t == 2) { S2CHECK(st1, 1); } else { WAITV(1); S2CHECK(st0, 2); } }
        if constexpr (ks >= e0s && ks < e0s + 16) { if constexpr ((ks - e0s) & 1) EPI_B((ks - e0s) >> 1); else EPI_A(acc[(t - 1) & 1], (ks - e0s) >> 1); }
        if constexpr (ks == e0s + 6) CSLD(1);
        if constexpr (ks == e0s + 16) AST(t - 1);
        if constexpr (t == 1) {
          if constexpr (ks == 0) { LDC4(st0, s2o, rs, S2OFF(0), 0); LDC4(st1, s2o, rs, S2OFF(1), 0); }
          if constexpr (ks >= 1 && ks < 8) LDX(ks - 1, xo2);
          if constexpr (ks == 8) RAWBAR();
          if constexpr (ks >= 9 && ks < 14) DMA2(ks < 12 ? ks - 9 : ks - 2);
          if constexpr (ks == 18) { WAITV(8); XCHECK(0, 4, xo2); }
          if constexpr (ks >= 19 && ks < 23) XW2(ks - 19);
        }
        if constexpr (t == 2) {
          if constexpr (ks == 0) { WAITV(6); XCHECK(4, 3, xo2); }
          if constexpr (ks >= 1 && ks < 4) XW2(ks + 3);
          if constexpr (ks == 4) RAWBAR();
          if constexpr (ks >= 5 && ks < 9) DMA2(ks - 2);
          if constexpr (ks == 17) { LDC2(fa, fao, rs, 0); LDC4(st0, s2o, rs, S2OFF(2), 0); LDC4(st1, s2o, rs, S2OFF(3), 0); }
          if constexpr (ks == 20) *(LAS u32x2*)lp(L_EF + w * 8) = u32x2{0u, 0u};
        }
        if constexpr (t == 3) {
          if constexpr (ks == 2) { TIE(fa); flagwait(fa, fao, rs, t0, t1); RAWBAR(); }
          if constexpr (ks == 3 || ks == 4) DMA2(9 + ks);
          if constexpr (ks >= 5) {
            if constexpr (ks < 9) { LDQC("=v", 2 * ks - 10, 0); LDQC("=v", 2 * ks - 9, 0); } else LDQC("=v", ks - 1, 0);
            if constexpr (ks == 27) LDQC("=v", 27, 0);
            if constexpr (ks >= 14) { LDQC("+v", 2 * ks - 28, 1); LDQC("+v", 2 * ks - 27, 1); }
          }
        }
      }
      SB();
    SEND
    TS(15 + t);
  SEND
  CSLD(0);
  S2CHECK(st1, 3);
#pragma unroll
  for (int i = 0; i < 8; ++i) { if (i == 4) CSLD(1); EPI_A(acc[1], i); EPI_B(i); }
  AST(3);
  TS(19);

  {
#pragma unroll
    for (int n = 0; n < 28; n += 4) LDQ4(n, 2);
    if (w < 6) WAITV(30); else WAITV(2);
    RAWBAR();
    if (w >= 4) __builtin_amdgcn_s_setprio(1);
    u32x4 Af[56];
    const u32 vb_ = kh * 57344 + (4 * (lane >> 5) + ((lane & 15) >> 2)) * 128 + ((hf ^ ((lane >> 3) & 1)) * 64) + ((((lane >> 4) ^ kh) & 1) * 32) + (lane & 3) * 8;
    const LAS char* vb = lp(vb_);
    const LAS char* vc = lp(vb_ ^ 64u);
#define LDV(n) Af[n] = (n) < 28 ? trd2(vc + (n) * 2048, 1024) : trd2(vb + ((n) - 28) * 2048, 1024)
    LDV(0); LDV(1); LDV(2);
    if (w >= 6) {
      const u32 po = bo - lane * 16 + (l28 >> 2) * 8192 + (l28 & 3) * 1024 + 1020;
      u32 pv;
#pragma unroll 1
      for (int it = 0; it < MAXSPIN; ++it) {
        LDC1(pv, po, rs); WAITV(0); TIE(pv);
        if (allz(pv == ~0u)) break;
      }
      TS(20);
    }
#pragma unroll
    for (int n = 0; n < 28; n += 4) LDQ4(n, 3);
    TS(21);
#pragma unroll
    for (int i = 0; i < 16; ++i) { acc[0][i] = 0.f; acc[1][i] = 0.f; }
    LAS char* ew = lp(L_EX + w * 4096 + lane * 16);
    const LAS char* er = lp(L_EX + (w ^ 1) * 4096 + lane * 16);
    const u32 qo = OFF_Q + ((P * 14 + g7) * 8 + w) * 2048 + lane * 16;
    const u32 ro = qo + hf * (7 * 8 * 2048);
    f32x4 p0, p1;
    u32x4 g0, g1;
#define EXW(a, ps) do { *(LAS f32x4*)(ew + (ps) * 2048) = f32x4{a[8], a[9], a[10], a[11]}; *(LAS f32x4*)(ew + (ps) * 2048 + 1024) = f32x4{a[12], a[13], a[14], a[15]}; } while (0)
#define EXF(ps) do { asm volatile("s_waitcnt lgkmcnt(0)" ::: "memory"); *(volatile LAS u32*)lp(L_EF + w * 8 + (ps) * 4) = 1u; } while (0)
#define EXR(ps) do { _Pragma("unroll 1") for (int it = 0; it < MAXSPIN; ++it) { \
      const u32 f_ = *(const volatile LAS u32*)lp(L_EF + (w ^ 1) * 8 + (ps) * 4); \
      if (__builtin_amdgcn_readfirstlane(f_)) break; } \
    asm volatile("" ::: "memory"); \
    p0 = *(const LAS f32x4*)(er + (ps) * 2048); p1 = *(const LAS f32x4*)(er + (ps) * 2048 + 1024); } while (0)
    SB();
    SFOR(n, 56)
      if constexpr (n < 28 && (n & 3) == 0) {
        WAITV(24 - n);
        u32 m_ = 0;
#pragma unroll
        for (int i = 0; i < 4; ++i) { TIE(Bq[n + i]); m_ = umax3(m_, Bq[n + i][0], Bq[n + i][1]); m_ = umax3(m_, Bq[n + i][2], Bq[n + i][3]); }
        if (RARE(m_ == ~0u)) {
#pragma unroll 1
          for (int it = 0; it < MAXSPIN; ++it) {
            WAITV(0);
#pragma unroll
            for (int i = n; i < 28; ++i) LDQ(i);
            WAITV(24 - n); m_ = 0;
#pragma unroll
            for (int i = 0; i < 4; ++i) { TIE(Bq[n + i]); m_ = umax3(m_, Bq[n + i][0], Bq[n + i][1]); m_ = umax3(m_, Bq[n + i][2], Bq[n + i][3]); }
            if (allz(m_ == ~0u)) break;
          }
        }
        if constexpr (n == 0) TS(22);
      }
      if constexpr (n + 3 < 56) LDV(n + 3);
      if constexpr (n < 28) acc[1] = mfma32(Af[n], Bq[n], acc[1]); else acc[0] = mfma32(Af[n], Bq[n - 28], acc[0]);
      if constexpr (n == 28) TS(23);
      if constexpr (n == 29) EXW(acc[1], 0);
      if constexpr (n == 30) EXF(0);
      if constexpr (n == 33) EXR(0);
      if constexpr (n == 34) {
        const u32x4 q0 = {pk2(acc[1][0] + p0[0], acc[1][1] + p0[1]), tA, pk2(acc[1][2] + p0[2], acc[1][3] + p0[3]), tB};
        const u32 a_ = qo + (hf ^ 1) * (7 * 8 * 2048);
        STC4(q0, a_, rs, 0);
      }
      if constexpr (n == 35) {
        const u32x4 q1 = {pk2(acc[1][4] + p1[0], acc[1][5] + p1[1]), tA, pk2(acc[1][6] + p1[2], acc[1][7] + p1[3]), tB};
        const u32 a_ = qo + (hf ^ 1) * (7 * 8 * 2048);
        STC4(q1, a_, rs, 1024);
        TS(24);
      }
      if constexpr (n == 50) { LDC4(g0, ro, rs, 0, 0); LDC4(g1, ro, rs, 0, 1024); }
      SB();
    SEND
    TS(25);
    EXW(acc[0], 1); EXF(1);
    float y[8];
#pragma unroll
    for (int e = 0; e < 4; ++e) { const u32 r = res[e]; y[2 * e] = acc[0][2 * e] + bf_lo(r); y[2 * e + 1] = acc[0][2 * e + 1] + bf_hi(r); }
    EXR(1);
#pragma unroll
    for (int e = 0; e < 4; ++e) { y[e] += p0[e]; y[4 + e] += p1[e]; }
    TS(26);
    WAITV(0); TIE(g0); TIE(g1);
    if (RARE(gbad(g0, tA, tB) || gbad(g1, tA, tB))) {
#pragma unroll 1
      for (int it = 0; it < MAXSPIN; ++it) {
        LDC4(g0, ro, rs, 0, 0); LDC4(g1, ro, rs, 0, 1024); WAITV(0); TIE(g0); TIE(g1);
        if (allz(gbad(g0, tA, tB) || gbad(g1, tA, tB))) break;
      }
    }
    TS(27);
    const u32 r0 = g0[0], r1 = g0[2], r2_ = g1[0], r3 = g1[2];
    const u32 x0 = pk2(y[0] + bf_lo(r0), y[1] + bf_hi(r0)), x1 = pk2(y[2] + bf_lo(r1), y[3] + bf_hi(r1));
    const u32 y0 = pk2(y[4] + bf_lo(r2_), y[5] + bf_hi(r2_)), y1 = pk2(y[6] + bf_lo(r3), y[7] + bf_hi(r3));
    const auto s0 = __builtin_amdgcn_permlane32_swap(x0, y0, false, false), s1 = __builtin_amdgcn_permlane32_swap(x1, y1, false, false);
    const int tok = (t2 < 2 ? 2 * P + t2 : 30 + 2 * P + t2) * 32 + (lane & 31);
    *(u32x4*)(out + (size_t)tok * D + 64 * g7 + 32 * hf + 16 * kh + 8 * (lane >> 5)) = u32x4{s0[0], s1[0], s0[1], s1[1]};
  }
  if (b == 0 && w == 0) { const u32x2 f = {t0, t1}; STF(f, zero, rs); }
  TSOUT();
}

extern "C" void kernel_launch(void* const* d_in, const int* in_sizes, int n_in, void* d_out, int out_size,
                              void* d_ws, size_t ws_size, hipStream_t stream) {
  (void)hipFuncSetAttribute((const void*)fused, hipFuncAttributeMaxDynamicSharedMemorySize, L_SIZE);
  fused<<<256, 512, L_SIZE, stream>>>((const u16*)d_in[0], (const u16*)d_in[1], (const u16*)d_in[2], (const u16*)d_in[3],
                                      (const u16*)d_in[4], (const u16*)d_in[5], (const u16*)d_in[7], (const u16*)d_in[8],
                                      (const u16*)d_in[9], (u16*)d_out, (char*)d_ws);
}
```

```cpp
#include <hip/hip_runtime.h>
#include <hip/hip_bf16.h>
#include <stdint.h>

typedef unsigned short u16;
typedef unsigned int u32;
typedef unsigned long long u64;
typedef u32 u32x2 __attribute__((ext_vector_type(2)));
typedef u32 u32x4 __attribute__((ext_vector_type(4)));
typedef short s16x4 __attribute__((ext_vector_type(4)));
typedef float f32x4 __attribute__((ext_vector_type(4)));
typedef float f32x16 __attribute__((ext_vector_type(16)));
typedef __bf16 bf16x8 __attribute__((ext_vector_type(8)));
typedef __bf16 bf16x2 __attribute__((ext_vector_type(2)));
#define DI __device__ __forceinline__
#define LAS __attribute__((address_space(3)))
#define SB() __builtin_amdgcn_sched_barrier(0)
template <int V> struct IC { static constexpr int v = V; };
template <int I, int N, class Fn> DI void sfor(Fn&& f) { if constexpr (I < N) { f(IC<I>{}); sfor<I + 1, N>(f); } }
#define SFOR(var, n) sfor<0, n>([&](auto ic_##var) __attribute__((always_inline)) { constexpr int var = decltype(ic_##var)::v;
#define SEND });

constexpr int S = 2048, D = 448, F = 1792;
constexpr u32 OFF_E = 0;
constexpr u32 OFF_FP = 4096;
constexpr u32 OFF_FA = OFF_FP + 16384;
constexpr u32 OFF_ST = OFF_FA + 20480;
constexpr u32 OFF_S2 = OFF_ST + 16384;
constexpr u32 SZ_SD = S * D * 2;
constexpr u32 OFF_U = OFF_S2 + 32768;
constexpr u32 OFF_X = OFF_U + 2 * SZ_SD;
constexpr u32 OFF_H = OFF_X + 2 * SZ_SD;
constexpr u32 OFF_A = OFF_H + SZ_SD;
constexpr u32 OFF_Q = OFF_A + S * F * 2;
constexpr u32 WS_END = OFF_Q + 16 * 14 * 8 * 1024;
constexpr int KC = 4160;
constexpr int L_F = 0;
constexpr int L_U = 8 * KC;
constexpr int US = 286 * 16;
constexpr int L_PE = 42496;
constexpr int L_G0 = 58880;
constexpr int L_JUNK = 157184;
constexpr int L_XB = 102400;
#define XCONT 0
constexpr int xco(int s) { return XCONT ? s * 1024 : s < 16 ? 40960 + s * 1024 : (s - 16) * 1024; }
constexpr int L_PV = 9216, L_PQ = L_PE;
constexpr int L_EX = 114688, L_EF = 159744;
constexpr int L_CS = 162816;
constexpr int L_SIZE = 163840;
constexpr int MAXSPIN = 1 << 18;
#define CT 1
#define XL 0
#define PHP 0
#define NE1 4
#define ROTA 7
#define ROTB 2
enum { K_W, K_WH, K_PH, K_XS, K_GL, K_GH, K_FH, K_PZ, K_FL, K_FA, K_HS, K_HS2, K_PQ, K_PQ2, K_X, K_X2, K_D2, K_S2, K_FAL, K_AS, K_NONE };
struct Ev { int step, k, a; };
#define NEP 0
#define XCK 8
#define TW 0
#define W2R 2
#define TWM 0
#define XW0 29
#define XL0 32
#define XD0 3
#define GL0 46
#define RQ 0
#define T3 0
#define T3S 0
#define WST 1
#define W2D 40
#define TW2 1
#define W_(s, i) {s, K_W, i},
#define WH_(s, i) {s, K_WH, i},
#define G4(s, k) {s, k, 0}, {s, k, 1}, {s, k, 2}, {s, k, 3},
#define X7(s) {s, K_X, 0}, {s, K_X, 1}, {s, K_X, 2}, {s, K_X, 3}, {s, K_X, 4}, {s, K_X, 5}, {s, K_X, 6},
#define PH3
#define FH0 {0, K_FH, 0},
#define FH6
#define SV 12
#define C1 1
#define LW1 1
#define B0 14
#define X2V 10
#define WL 0
#define WLATE W_(26, 24) W_(26, 25) W_(26, 26) W_(26, 27) {26, K_PQ2, 0},
#define SCH_C G4(-1, K_GL) W_(-1, 4) W_(-1, 5) W_(-1, 6) PH3 FH0 W_(4, 7) W_(6, 8) FH6 W_(8, 9) \
  WH_(9, 10) WH_(9, 11) WH_(9, 12) WH_(9, 13) WH_(9, 14) WH_(9, 15) {9, K_HS, 0}, {9, K_PQ, 0}, G4(10, K_GH) {14, K_FL, 0}, \
  W_(19, 16) W_(19, 17) W_(19, 18) W_(20, 19) W_(20, 20) W_(21, 21) W_(21, 22) W_(22, 23) {25, K_HS2, 0}, {25, K_X, 0}, {25, K_X, 1}, {25, K_X, 2}, {25, K_X, 3}, {25, K_X, 4}, {25 + (XD == 2), K_X, 5}, {25 + (XD == 2), K_X, 6}, \
  WLATE
#define LWS_ {0, 1, 2, 3, 14, 14, 15, 15, 16, 16, 17, 17, 19, 20, 21, 22, 26, 26, 26, 26, 26, 26, 26, 26, 100 + B0, 101 + B0, 102 + B0, 103 + B0}
#define CSS_ {4, 5, 6, 7, 18, 18, 19, 19, 20, 20, 21, 21, 23, 23, 25, 25, 27, 27, 27, 27, 27, 27, 27, 27, 108 + B0, 109 + B0, 110 + B0, 111 + B0}
#define XPH 18
#define SCH_XS
#define SCH_PH
#define SCH_M2
#define SCHED SCH_XS SCH_PH SCH_M2 SCH_C \
  SCH_G
#define PS 101
#define XS 118
#define XC 8
#define E1 12
#define XI 2
#define XD 2
#define D2E {141 + B1, K_D2, 2}, {143 + B1, K_D2, 11},
#define D2L
#define NB12 1
#define FCK 2
#define B1 (XC + 4)
#define SCH_G {PS, K_PZ, 0}, {PS + 1, K_PZ, 1}, {PS + 2, K_PZ, 2}, {PS + 3, K_PZ, 3}, \
  {XS, K_X2, 0}, {XS + XI, K_X2, 1}, {XS + 2 * XI, K_X2, 2}, {XS + 3 * XI, K_X2, 3}, {XS + 4 * XI, K_X2, 4}, {XS + 5 * XI, K_X2, 5}, {XS + 6 * XI, K_X2, 6}, {XS + 6 * XI + 1, K_S2, 0}, \
  {128 + B1, K_FA, 0}, {129 + B1, K_D2, 7}, {131 + B1, K_D2, 8}, {133 + B1, K_D2, 9}, {135 + B1, K_D2, 10}, {136 + B1, K_S2, 1}, {137 + B1, K_D2, 0}, {139 + B1, K_D2, 1}, \
  D2E {144 + (E1 > B1 ? E1 : B1), K_AS, 0}, D2L SCH_T
#define SP2 0
#define PZE 0
#define NSP 4
#define SPZ 0
#define B2 (XCONT ? 14 : 2)
#define SCH_T \
  {159, K_D2, 3}, {161, K_D2, 4}, {163, K_D2, 5}, {165, K_D2, 6}, {169, K_FAL, 0}, {171, K_S2, 2}, {173, K_S2, 3}, {174, K_AS, 1}
constexpr Ev SCH[] = {SCHED};
constexpr int NEV = sizeof(SCH) / sizeof(Ev);
constexpr int evn(int k, bool hi) { return (SPZ || PZE) && (k == K_PQ || k == K_PQ2) ? 0 : k == K_WH ? (hi ? 2 : 0) : k == K_HS || k == K_PQ || k == K_GL ? !hi : k == K_HS2 || k == K_PQ2 || k == K_FL || k == K_GH ? hi : k != K_NONE; }
constexpr int evi(int k, int a) { for (int e = 0; e < NEV; ++e) if ((SCH[e].k == k || (k == K_W && SCH[e].k == K_WH)) && SCH[e].a == a) return e; return -1; }
constexpr int evs(int step) { int e = 0; while (e < NEV && SCH[e].step < step) ++e; return e; }
constexpr int evc(int e0, int e1, bool hi) {
  if (e0 >= 0 && evn(SCH[e0].k, hi) == 0) return 63;
  int n = 0; for (int e = e0 + 1; e < e1; ++e) n += evn(SCH[e].k, hi);
  return e0 < e1 ? (n < 63 ? n : 63) : -1;
}
constexpr int imin(int a, int b) { return a < b ? a : b; }
constexpr int wslot(int i) { return L_G0 + (i % 12) * 8192; }
constexpr int LWS[28] = LWS_, CSS[28] = CSS_;
constexpr int BARS[] = {-1, 9, 14, 19, 25, C1 ? 26 : 25, 100, 100 + B0, 1000};
constexpr int nbar(int s) { int i = 0; while (BARS[i] <= s) ++i; return BARS[i]; }
constexpr int wcnt(int s, int e1, bool hi) { int n = 63; for (int i = NE1; i < 28; ++i) if (LWS[i] >= s && LWS[i] < nbar(s)) n = imin(n, evc(evi(K_W, i), e1, hi)); return n; }
constexpr bool slots_() { for (int i = 12; i < 28; ++i) if (SCH[evi(K_W, i)].step < nbar(LWS[i - 12])) return false;
                          for (int i = 0; i < 28; ++i) if (CSS[i] <= LWS[i] || (!LW1 && LWS[i] >= 100 + B0) || (LWS[i] >= 100 && LWS[i] + 4 > 100 + i)) return false; return true; }
static_assert(slots_());
#define PD1 3
#define PD2 3
#define PZD 0
#define TLS 0
#define PLS 0
#define QE 3
#define PRI 4
#define PRO 0
constexpr int GB[] = {100, 100 + B0, NB12 ? 100 + B0 : 130, 128 + B1, 156 + B2, XCONT ? 156 + B2 : 186, 212};
constexpr int prio_(int gs, int off) { if (PLS && gs >= PLS) return off < 0 ? 0 : 3; if (off < 0) off = 0; int i = 0; while (GB[i + 1] <= gs) ++i; const int j = (PRI * (gs - GB[i]) + off) / (GB[i + 1] - GB[i]); return j >= PRI ? 0 : PRI - 1 - j; }
#define WAITB(s) WAIT2(wcnt(s, evs(s), false), wcnt(s, evs(s), true))
constexpr bool sorted_() { for (int e = 1; e < NEV; ++e) if (SCH[e].step < SCH[e - 1].step) return false; return true; }
static_assert(sorted_());

#define TSDECL
#define RCNT(i)
#define TS(i)
#define TSF(i)
#define TSG(i)
#define TSC(c, i)
#define TSOUT()

DI float bf_lo(u32 v) { return __uint_as_float(v << 16); }
DI float bf_hi(u32 v) { return __uint_as_float(v & 0xffff0000u); }
DI u32 pk2(float lo, float hi) { bf16x2 r; r[0] = (__bf16)lo; r[1] = (__bf16)hi; return __builtin_bit_cast(u32, r); }
DI float rbf(float x) { return (float)(__bf16)x; }
DI u32 plo(u32 a, u32 b) { return __builtin_amdgcn_perm(b, a, 0x05040100u); }
DI u32 phi(u32 a, u32 b) { return __builtin_amdgcn_perm(b, a, 0x07060302u); }
DI f32x16 mfma32(u32x4 a, u32x4 b, f32x16 c) {
  return __builtin_amdgcn_mfma_f32_32x32x16_bf16(__builtin_bit_cast(bf16x8, a), __builtin_bit_cast(bf16x8, b), c, 0, 0, 0);
}
DI f32x4 mfma16(u32x4 a, u32x4 b, f32x4 c) {
  return __builtin_amdgcn_mfma_f32_16x16x32_bf16(__builtin_bit_cast(bf16x8, a), __builtin_bit_cast(bf16x8, b), c, 0, 0, 0);
}
DI float dot2(u32 a, u32 b, float c) { return __builtin_amdgcn_fdot2_f32_bf16(__builtin_bit_cast(bf16x2, a), __builtin_bit_cast(bf16x2, b), c, false); }
DI u32x2 trd(const LAS char* p) { return __builtin_bit_cast(u32x2, __builtin_amdgcn_ds_read_tr16_b64_v4i16((LAS s16x4*)p)); }
DI u32x4 trd2(const LAS char* p, int d) { const u32x2 a = trd(p), b = trd(p + d); return u32x4{a[0], a[1], b[0], b[1]}; }
DI u32 umax3(u32 a, u32 b, u32 c) { const u32 m = a > b ? a : b; return m > c ? m : c; }
DI u32 nop32(u32 x) { return x < 0xfffffffeu ? x : 0xfffffffeu; }
DI bool allz(bool bad) { return __builtin_amdgcn_ballot_w64(bad) == 0; }
#define RARE(bad) __builtin_expect(!allz(bad), 0)
DI u32x4 mksrd(const void* p, u32 bytes) { const u64 a = (u64)p; return u32x4{(u32)a, (u32)(a >> 32) & 0xffffu, bytes, 0x00020000u}; }

#define LD1(d, vo, srd, off) asm volatile("buffer_load_dword %0, %1, %2, 0 offen offset:%3" : "=v"(d) : "v"(vo), "s"(srd), "i"(off) : "memory")
#define LD2(d, vo, srd, off) asm volatile("buffer_load_dwordx2 %0, %1, %2, 0 offen offset:%3" : "=v"(d) : "v"(vo), "s"(srd), "i"(off) : "memory")
#define LD4(d, vo, srd, off) asm volatile("buffer_load_dwordx4 %0, %1, %2, 0 offen offset:%3" : "=v"(d) : "v"(vo), "s"(srd), "i"(off) : "memory")
#define LDC1(d, vo, srd) asm volatile("buffer_load_dword %0, %1, %2, 0 offen sc1" : "=v"(d) : "v"(vo), "s"(srd) : "memory")
#define LDC2(d, vo, srd, so) asm volatile("buffer_load_dwordx2 %0, %1, %2, %3 offen sc1" : "=v"(d) : "v"(vo), "s"(srd), "s"(so) : "memory")
#define LDC4(d, vo, srd, so, off) asm volatile("buffer_load_dwordx4 %0, %1, %2, %3 offen offset:%4 sc1" : "=v"(d) : "v"(vo), "s"(srd), "s"(so), "i"(off) : "memory")
#define STC4(v, vo, srd, off) asm volatile("buffer_store_dwordx4 %0, %1, %2, 0 offen offset:%3 sc1\n\ts_nop 1" :: "v"(v), "v"(vo), "s"(srd), "i"(off) : "memory")
#define STF(v, vo, srd) asm volatile("s_mov_b64 exec, 1\n\tbuffer_store_dwordx2 %0, %1, %2, 0 offen sc1\n\ts_mov_b64 exec, -1" :: "v"(v), "v"(vo), "s"(srd) : "memory")
#define DMA(la, vo, srd, so) asm volatile("s_mov_b32 m0, %0\n\ts_nop 0\n\tbuffer_load_dwordx4 %1, %2, %3 offen lds" :: "s"(la), "v"(vo), "s"(srd), "s"(so) : "memory")
#define NTP 1
#define NTW 0
#define NTS " nt"
#define NTR ""
#define DMAC(la, vo, srd, so) asm volatile("s_mov_b32 m0, %0\n\ts_nop 0\n\tbuffer_load_dwordx4 %1, %2, %3 offen sc1 lds" :: "s"(la), "v"(vo), "s"(srd), "s"(so) : "memory")
#define DMAN(la, vo, srd, so) asm volatile("s_mov_b32 m0, %0\n\ts_nop 0\n\tbuffer_load_dwordx4 %1, %2, %3 offen nt lds" :: "s"(la), "v"(vo), "s"(srd), "s"(so) : "memory")
#define DMAP(la, vo, srd) asm volatile("s_mov_b32 m0, %0\n\ts_nop 0\n\tbuffer_load_dwordx4 %1, %2, 0 offen" NTS " lds" :: "s"(la), "v"(vo), "s"(srd) : "memory")
#define LD4P(d, vo, srd) asm volatile("buffer_load_dwordx4 %0, %1, %2, 0 offen" NTS : "=v"(d) : "v"(vo), "s"(srd) : "memory")
#define DMA1(la, vo, srd) asm volatile("s_mov_b32 m0, %0\n\ts_nop 0\n\tbuffer_load_dword %1, %2, 0 offen lds" :: "s"(la), "v"(vo), "s"(srd) : "memory")
#define WAITV(n) asm volatile("s_waitcnt vmcnt(%0)" :: "i"(n) : "memory")
#define WAIT2(l, h) do { static_assert((l) >= 0 && (h) >= 0); if constexpr ((l) == (h)) WAITV(l); else { if (w < 4) WAITV(l); else WAITV(h); } } while (0)
#define WAITE(k, a, step) WAIT2(evc(evi(k, a), evs(step), false), evc(evi(k, a), evs(step), true))
#define WAITE2(k, a, k2, a2, step) WAIT2(imin(evc(evi(k, a), evs(step), false), evc(evi(k2, a2), evs(step), false)), \
                                         imin(evc(evi(k, a), evs(step), true), evc(evi(k2, a2), evs(step), true)))
#define LDC4P(d, vo, srd, so, off) asm volatile("buffer_load_dwordx4 %0, %1, %2, %3 offen offset:%4 sc1" : "+v"(d) : "v"(vo), "s"(srd), "s"(so), "i"(off) : "memory")
#define TIE(x) asm volatile("" : "+v"(x))
DI void bar() { asm volatile("s_waitcnt lgkmcnt(0)\n\ts_barrier" ::: "memory"); }
DI bool gbad(u32x4 g, u32 tA, u32 tB) { return g[1] != tA || g[3] != tB; }
DI void flagwait(u32x2 f, u32 vo, u32x4 srd, u32 t0, u32 t1) {
  if (RARE(f[0] != t0 || f[1] != t1)) {
#pragma unroll 1
    for (int it = 0; it < MAXSPIN; ++it) {
      __builtin_amdgcn_s_sleep(1);
      LDC2(f, vo, srd, 0); WAITV(0); TIE(f);
      if (allz(f[0] != t0 || f[1] != t1)) break;
    }

  }
}

DI LAS char* lp(u32 a) { return (LAS char*)(size_t)a; }
DI u32 opq(u32 a) { TIE(a); return a; }
#define RAWBAR() asm volatile("s_barrier" ::: "memory")

__global__ __launch_bounds__(512, 2) void fused(const u16* __restrict__ q, const u16* __restrict__ k, const u16* __restrict__ v,
                                                const u16* __restrict__ shk, const u16* __restrict__ ssk, const u16* __restrict__ ssd,
                                                const u16* __restrict__ w1, const u16* __restrict__ w3, const u16* __restrict__ w2,
                                                u16* __restrict__ out, char* ws) {
  extern __shared__ __attribute__((aligned(16))) char lds_[];
  const int tid = threadIdx.x, lane = tid & 63;
  const int w = __builtin_amdgcn_readfirstlane(tid >> 6);
  const int b = blockIdx.x, xcd = b & 7, bi = b >> 3;
  const bool spare = bi >= 28;
  TSDECL; TS(0);
  const u32x4 rs = mksrd(ws, WS_END), rq = mksrd(q, SZ_SD), rk = mksrd(k, SZ_SD), rv = mksrd(v, SZ_SD), rh = mksrd(shk, D * 8),
              rf = mksrd(ssk, SZ_SD), rw = mksrd((w & 1) ? w3 : w1, D * F * 2), r2 = mksrd(w2, D * F * 2);
  const int jc = spare ? 0 : xcd * 28 + bi;
  const int P = (xcd >> 1) * 4 + (bi & 3), G = (xcd & 1) * 7 + (spare ? bi - 28 : bi >> 2);
  const int rot = (ROTA * (bi & 3) + ROTB * (xcd >> 1) + (spare ? 14 : 0)) % 28;

  const int tb = xcd * 8 + (bi >> 2), cq = bi & 3;
  const int tq = lane & 7;
  u32x4 kin, sk, sq[7]; u32x2 knx, e0;
  const u32 zero = 0;
  const int cp = 8 * w + (lane >> 3);
  {
    const int c = (lane & 15) ^ w;
    const u32 o = (tb * 32 + 4 * w + (lane >> 4)) * (D * 2) + cq * 224 + c * 16;
    const u32 ov = c < 14 ? o : 0x80000000u;
    const u32 ok = (c < 14 && (tb | w)) ? o - 4 * (D * 2) : 0x80000000u;
    DMAP(w * 1024, ok, rk); DMAP(L_PV + w * 1024, ov, rv);
    LDC2(e0, zero, rs, 0);
    const u32 so = (cq * 112 + 2 * cp) * 8;
    LD4(sk, so, rh, 0);
    const u32 ot = (lane & 15) < 14 ? (tb * 32 + 28 + (lane >> 4)) * (D * 2) + cq * 224 + (lane & 15) * 16 : 0x80000000u;
    const u32 vo = (tb * 32 + 8 * cq + (lane >> 3)) * (D * 2) + (lane & 7) * 16;
    asm volatile("s_cmp_eq_u32 %2, 7\n\ts_cbranch_scc0 .Lp%=\n\ts_movk_i32 m0, 0x2000\n\ts_nop 0\n\tbuffer_load_dwordx4 %0, %1, 0 offen" NTS " lds\n.Lp%=:"
                 :: "v"(ot), "s"(rk), "s"(w) : "memory", "scc");
    RAWBAR();
    DMAP(L_PQ + w * 1024, ov, rq);
    asm volatile("s_cmp_eq_u32 %9, 7\n\ts_cbranch_scc0 .Lp%=\n\t"
                 "buffer_load_dwordx4 %0, %7, %8, 0 offen" NTR "\n\tbuffer_load_dwordx4 %1, %7, %8, 0 offen offset:128" NTR "\n\t"
                 "buffer_load_dwordx4 %2, %7, %8, 0 offen offset:256" NTR "\n\tbuffer_load_dwordx4 %3, %7, %8, 0 offen offset:384" NTR "\n\t"
                 "buffer_load_dwordx4 %4, %7, %8, 0 offen offset:512" NTR "\n\tbuffer_load_dwordx4 %5, %7, %8, 0 offen offset:640" NTR "\n\t"
                 "buffer_load_dwordx4 %6, %7, %8, 0 offen offset:768" NTR "\n.Lp%=:"
                 : "=&v"(sq[0]), "=&v"(sq[1]), "=&v"(sq[2]), "=&v"(sq[3]), "=&v"(sq[4]), "=&v"(sq[5]), "=&v"(sq[6])
                 : "v"(vo), "s"(rq), "s"(w) : "memory", "scc");
  }
  const u32 wvo = (lane >> 4) * (F * 2) + G * 256 + (((lane & 15) ^ ((4 * (w & 1) + (lane >> 4)) << 1)) << 4);
  const u32 wso = (w >> 1) * 4 * (F * 2) + rot * 16 * (F * 2);
  const u32 fvo = jc * (2 * S * 2) + tid * 16;
#define WSO(i) (wso + (i) * 16 * (F * 2) - ((i) + rot >= 28 ? 28 * 16 * (F * 2) : 0))
#define DMAW(i) do { if ((i) < NTW) DMAN(wslot(i) + w * 1024, wvo, rw, WSO(i)); else DMA(wslot(i) + w * 1024, wvo, rw, WSO(i)); } while (0)
#define DMAW4(i) DMA(wslot(i) + w * 1024 - 4096, wvo, rw, WSO(i) - 8 * (F * 2))
#define MKTAG() do { u32 l_, h_; asm volatile("v_readfirstlane_b32 %0, %2\n\tv_readfirstlane_b32 %1, %3" : "=s"(l_), "=s"(h_) : "v"(e0[0]), "v"(e0[1])); \
    const u64 tg = (((u64)h_ << 32) | l_) + 1; t0 = (u32)tg; t1 = (u32)(tg >> 32); tA = t0; tB = t0 ^ t1 ^ 0x9E3779B9u; } while (0)
  LD4P(kin, fvo, rf);
  { const u32 nx = lane == 63 && (w & 3) != 3 ? fvo + 16 : 0x80000000u; LD2(knx, nx, rf, 0); }
  if constexpr (CT) {
    u64 pc; asm volatile("s_getpc_b64 %0" : "=s"(pc));
    const u32x4 rc = mksrd((const void*)pc, 24576);
    const u32 co = lane == 0 ? (bi * 8 + w) * 128 : 0x80000000u;
    DMA1(L_JUNK, co, rc);
  }
  if constexpr (TW) {
    const u32 l = (bi * 4 + (w >> 1)) * 13 + lane, r = (l * 4682) >> 16;
    const u32 o = lane < 13 && l < 1568 ? ((xcd >> 1) * 112 + r) * (F * 2) + (xcd & 1) * 1792 + (l - 14 * r) * 128 : 0x80000000u;
    DMA1(L_JUNK, o, rw);
  }
  const u32x4 ones = {~0u, ~0u, ~0u, ~0u};
  const u32 ao = OFF_A + (P * 448 + G * 8 + w) * 1024 + lane * 16;
#define QSA (OFF_Q + ((P * 14 + (G >= 7 ? G - 7 : G + 7)) * 8 + w) * 1024 + lane * 16)
#define PZ(t) do { const u32 a_ = (t) < 4 ? ao + (t) * 112 * 1024 : QSA; STC4(ones, a_, rs, 0); } while (0)
  constexpr int PZ1 = PZE == 1 ? 5 : 0;
  if constexpr (PZE == 1) {
#pragma unroll
    for (int t = 0; t < 5; ++t) { const u32 a_ = spare ? 0x80000000u : t < 4 ? ao + t * 112 * 1024 : QSA; STC4(ones, a_, rs, 0); }
  }
  u32 t0, t1, tA, tB, xso_ = 0x80000000u;
  u32x4 xg0 = {0u, 0u, 0u, 0u}, xg1 = {0u, 0u, 0u, 0u};
#define XST() do { STC4(xg0, xso_, rs, 0); STC4(xg1, xso_, rs, 128); } while (0)
  if (w < 7) {
    WAITV(3 + TW + CT + PZ1);
    TIE(sk);
    TS(1);
#pragma unroll
    for (int i = 0; i < NEP; ++i) DMAW(i);
    RAWBAR();
    const int c = 2 * w + (lane >> 5);
    const LAS char* pa = lp(tq * 1024 + ((c ^ tq) << 4) + ((lane >> 3) & 3) * 4);
    const LAS char* pb = lp(tq * 1024 + ((c ^ ((tq + 1) & 7)) << 4) + ((lane >> 3) & 3) * 4);
    u32 kk[7], vv[4], qq[4];
#pragma unroll
    for (int j = 0; j < 7; ++j) kk[j] = *(const LAS u32*)((j < 3 ? pa : pb) + (j + 1) * 256);
#pragma unroll
    for (int j = 0; j < 4; ++j) vv[j] = *(const LAS u32*)(pa + L_PV + j * 256);
    MKTAG();
    const u32 s0 = sk[0], s1 = sk[1], s2 = sk[2], s3 = sk[3];
    const float a0 = bf_lo(s0), a1 = bf_hi(s0), a2 = bf_lo(s1), a3 = bf_hi(s1);
    const float c0 = bf_lo(s2), c1 = bf_hi(s2), c2 = bf_lo(s3), c3 = bf_hi(s3);
    float u0[4], u1[4];
#pragma unroll
    for (int j = 0; j < 4; ++j) {
      u0[j] = (bf_lo(kk[j]) * a3 + bf_lo(kk[j + 1]) * a2 + bf_lo(kk[j + 2]) * a1 + bf_lo(kk[j + 3]) * a0) * bf_lo(vv[j]);
      u1[j] = (bf_hi(kk[j]) * c3 + bf_hi(kk[j + 1]) * c2 + bf_hi(kk[j + 2]) * c1 + bf_hi(kk[j + 3]) * c0) * bf_hi(vv[j]);
    }
    const u32 uo = OFF_U + tb * 57344 + (cq * 112 + 2 * cp) * 128 + tq * 16;
    { const u32x4 g = {pk2(u0[3], u0[2]), tA, pk2(u0[1], u0[0]), tB}; STC4(g, uo, rs, 0); }
    { const u32x4 g = {pk2(u1[3], u1[2]), tA, pk2(u1[1], u1[0]), tB}; STC4(g, uo, rs, 128); }
    TS(2);
    WAITV(4 + TW + CT + PZ1 + NEP);
    RAWBAR();
#pragma unroll
    for (int j = 0; j < 4; ++j) qq[j] = *(const LAS u32*)(pa + L_PQ + j * 256);
    xso_ = uo + (OFF_X - OFF_U);
    xg0 = u32x4{plo(qq[0], qq[1]), tA, plo(qq[2], qq[3]), tB}; xg1 = u32x4{phi(qq[0], qq[1]), tA, phi(qq[2], qq[3]), tB};
    if constexpr (XL == 0) XST();
  } else {
    WAITV(10 + TW + CT + PZ1);
    TS(1);
#pragma unroll
    for (int i = 0; i < NEP; ++i) DMAW(i);
    RAWBAR();
    MKTAG();
    WAITV(9 + TW + CT + PZ1 + NEP);
    TS(2);
    RAWBAR();
    WAITV(2 + TW + CT + PZ1 + NEP);
#pragma unroll
    for (int c = 0; c < 7; ++c) TIE(sq[c]);
    float s = 0.f, ss = 0.f, s_ = 0.f, ss_ = 0.f;
#pragma unroll
    for (int c = 0; c < 7; ++c) {
      const u32 x0 = sq[c][0], x1 = sq[c][1], x2 = sq[c][2], x3 = sq[c][3];
      s = dot2(x0, 0x3f803f80u, s); ss = dot2(x0, x0, ss); s_ = dot2(x1, 0x3f803f80u, s_); ss_ = dot2(x1, x1, ss_);
      s = dot2(x2, 0x3f803f80u, s); ss = dot2(x2, x2, ss); s_ = dot2(x3, 0x3f803f80u, s_); ss_ = dot2(x3, x3, ss_);
    }
    s += s_; ss += ss_;
#define DPPADD(x, ctl) x += __uint_as_float(__builtin_amdgcn_mov_dpp(__float_as_uint(x), ctl, 0xf, 0xf, true))
    DPPADD(s, 0xb1); DPPADD(ss, 0xb1); DPPADD(s, 0x4e); DPPADD(ss, 0x4e); DPPADD(s, 0x141); DPPADD(ss, 0x141);
    const float mean = s * (1.0f / D);
    const float var = rbf(fmaxf(ss * (1.0f / D) - mean * mean, 0.f));
    const u32 st = pk2(mean, sqrtf(rbf(var + 1e-5f)));
    const u32 sb = __builtin_amdgcn_mov_dpp(st, 0x108, 0xf, 0xf, true);
    { const u32x4 g = {st, tA, sb, tB};
      const u32 so = (lane & 15) == 0 ? OFF_ST + ((lane >> 4) & 1) * 8192 + (tb * 8 + 2 * cq + (lane >> 5)) * 16 : 0x80000000u; STC4(g, so, rs, 0); }
  }

  if (spare) {
    if constexpr (XL != 0) XST();
    WAITV(0); SB();
    const int lane = opq(tid & 63);
#define TLT() do { const u32 j_ = (bi - 28) * 8 + w, i_ = j_ * 5 + lane; \
      const u32 a_ = lane < 5 && i_ < 140 ? OFF_A + i_ * 65536 : 0x80000000u, o_ = lane == 0 && j_ < 28 ? j_ * 65536 : 0x80000000u; \
      const u32x4 ro_ = mksrd(out, SZ_SD); DMA1(L_JUNK, a_, rs); DMA1(L_JUNK, o_, ro_); } while (0)
    if constexpr (TLS == 1) TLT();
    if constexpr (W2D > 0) __builtin_amdgcn_s_sleep(W2D);
    if constexpr (TLS == 2) TLT();
    if constexpr (TW2) {
#pragma unroll
      for (int i = 0; i < 4; ++i) {
        const u32 l = (((bi - 28) * 8 + ((w + W2R * (xcd >> 1)) & 7)) * 4 + i) * 49 + lane;
        const u32 o = lane < 49 ? (xcd & 1) * (896 * D * 2) + l * 128 : 0x80000000u;
        DMA1(L_JUNK, o, r2);
      }
    }
    const int s = xcd * 4 + (bi - 28);
    if constexpr (SPZ) {
      if constexpr (PZD > 0) __builtin_amdgcn_s_sleep(PZD);
      const u32 pa = OFF_A + (s >> 1) * (448 * 1024) + ((s & 1) * 8 + w) * (NSP * 7168) + lane * 16, pq = OFF_Q + (s * 8 + w) * 7168 + lane * 16;
#pragma unroll
      for (int i = 0; i < 7 * NSP; ++i) { const u32 a_ = pa + i * 1024; STC4(ones, a_, rs, 0); }
      if constexpr (SPZ == 1) {
#pragma unroll
        for (int i = 0; i < 7; ++i) { const u32 a_ = pq + i * 1024; STC4(ones, a_, rs, 0); }
      }
      WAITV(0);
      const u32x2 f = {t0, t1}; const u32 fo = OFF_FA + 16 * 112 * 8 + (s * 8 + w) * 8; STF(f, fo, rs);
      TS(8);
    }
    const int b3 = w < 4 ? w + 24 : w + 16;
    LAS char* blk = lp(65536 + w * 4096);
    const LAS char* xb = blk + (8 * (lane >> 5) + ((lane & 15) >> 2)) * 64 + ((lane >> 4) & 1) * 32 + (lane & 3) * 8;
    bar();
#pragma unroll 1
    for (int hf = 0; hf < 2; ++hf) {
      const int tbs = s + 32 * hf;
      {
        const int bq = (lane >> 3) & 3;
        const u32 fo = OFF_FP + ((tbs >> 3) * 256 + (bq == 3 ? b3 : w + 8 * bq) * 8 + (lane & 7)) * 8;
        u32x2 f; LDC2(f, fo, rs, 0); WAITV(0); TIE(f);
        flagwait(f, fo, rs, t0, t1);
      }
      u32x4 xs[4];
      const u32 xo = OFF_H + (tbs * 28 + w) * 1024 + lane * 16, xo3 = OFF_H + (tbs * 28 + b3) * 1024 + lane * 16;
#pragma unroll 1
      for (int it = 0; it < MAXSPIN; ++it) {
        LDC4(xs[0], xo, rs, 0, 0); LDC4(xs[1], xo, rs, 8192, 0); LDC4(xs[2], xo, rs, 16384, 0); LDC4(xs[3], xo3, rs, 0, 0);
        WAITV(0);
        u32 m = 0;
#pragma unroll
        for (int i = 0; i < 4; ++i) { TIE(xs[i]); m = umax3(m, xs[i][0], xs[i][1]); m = umax3(m, xs[i][2], xs[i][3]); }
        if (allz(m == ~0u)) break;
      }
      if (hf == 0) TS(4); else TS(6);
#pragma unroll
      for (int i = 0; i < 4; ++i) *(LAS u32x4*)(blk + i * 1024 + lane * 16) = xs[i];
      float sm = 0.f, ss = 0.f, sm_ = 0.f, ss_ = 0.f;
#pragma unroll
      for (int i = 0; i < 4; ++i) {
        u32x4 x = trd2(xb + i * 1024, 256);
        if (i == 3 && w >= 4) x = u32x4{0u, 0u, 0u, 0u};
        const u32 x0 = x[0], x1 = x[1], x2 = x[2], x3 = x[3];
        sm = dot2(x0, 0x3f803f80u, sm); ss = dot2(x0, x0, ss); sm_ = dot2(x1, 0x3f803f80u, sm_); ss_ = dot2(x1, x1, ss_);
        sm = dot2(x2, 0x3f803f80u, sm); ss = dot2(x2, x2, ss); sm_ = dot2(x3, 0x3f803f80u, sm_); ss_ = dot2(x3, x3, ss_);
      }
      *(LAS u32x2*)lp(32768 + hf * 4096 + (lane * 8 + w) * 8) = u32x2{__float_as_uint(sm + sm_), __float_as_uint(ss + ss_)};
      bar();
      if (w == 0) {
        float a = 0.f, c = 0.f;
#pragma unroll
        for (int i = 0; i < 4; ++i) {
          const u32x4 p = *(const LAS u32x4*)lp(32768 + hf * 4096 + lane * 64 + i * 16);
          const u32 p0 = p[0], p1 = p[1], p2 = p[2], p3 = p[3];
          a += __uint_as_float(p0) + __uint_as_float(p2); c += __uint_as_float(p1) + __uint_as_float(p3);
        }
        a += __shfl_xor(a, 32); c += __shfl_xor(c, 32);
        const float mean = a * (1.0f / D);
        const float var = fmaxf(c * (1.0f / D) - mean * mean, 0.f);
        if (lane < 32) {
          const u32x4 g = {__float_as_uint(-mean), tA, __float_as_uint(var + 1e-5f), tB};
          const u32 so = OFF_S2 + (tbs * 32 + lane) * 16; STC4(g, so, rs, 0);
        }
      }
      if (hf == 0) TS(5); else TS(7);
      if (SP2 && hf == 0) {
        const u32 pa = OFF_A + (s >> 1) * (448 * 1024) + 112 * 1024 + ((s & 1) * 8 + w) * 7168 + lane * 16;
#pragma unroll
        for (int i = 0; i < 7; ++i) { const u32 a_ = pa + i * 1024; STC4(ones, a_, rs, 0); }
        WAITV(0);
        const u32x2 f = {t0, t1}; const u32 fo = OFF_FA + 16 * 112 * 8 + 2048 + (s * 8 + w) * 8; STF(f, fo, rs);
        TS(9);
      }
    }
    TSOUT();
    return;
  }

  TSF(26);
  const int etb = tid >> 3;
  const u32 ho = OFF_H + etb * 28672 + jc * 128 + (tq & 1) * 64 + (tq >> 1) * 16;
  if constexpr (PHP == 0) STC4(ones, ho, rs, 0);
#pragma unroll
  for (int i = NEP; i < NE1; ++i) DMAW(i);
  if constexpr (XL == 1) XST();
  if constexpr (PHP == 1) STC4(ones, ho, rs, 0);
  if constexpr (PZE == 2) { PZ(0); PZ(1); PZ(2); PZ(3); PZ(4); }
  constexpr int NAU = (PZE == 2 ? 5 : 0) + (PHP < 2) + NE1 - NEP + (XL == 1 ? 2 : 0);
  const u32 dd = *(const u32*)(ssd + 2 * jc);
  const u32 go = etb * 57344 + jc * 256 + tq * 16, so_ = tid * 16;
  u32x4 gu0, gu1, gx0, gx1, gs0, gs1;
#define LDGU(LD) do { LD(gu0, go, rs, OFF_U, 0); LD(gu1, go, rs, OFF_U, 128); } while (0)
#define LDGX(LD) do { LD(gx0, go, rs, OFF_X, 0); LD(gx1, go, rs, OFF_X, 128); LD(gs0, so_, rs, OFF_ST, 0); LD(gs1, so_, rs, OFF_ST + 8192, 0); } while (0)
#define GBADU() (gbad(gu0, tA, tB) || gbad(gu1, tA, tB))
#define GBADX() (gbad(gx0, tA, tB) || gbad(gx1, tA, tB) || gbad(gs0, tA, tB) || gbad(gs1, tA, tB))
#define TIEU() do { TIE(gu0); TIE(gu1); } while (0)
#define TIEX() do { TIE(gx0); TIE(gx1); TIE(gs0); TIE(gs1); } while (0)
  TS(3);
  if (w < 7) WAITV(TW + CT + PZ1 + NEP + 2 + (XL == 0 ? 2 : 0) + NAU); else WAITV(TW + CT + PZ1 + NEP + 1 + NAU);
  TIE(kin); TIE(knx);
  {
    const int c = tid >> 8, t8 = tid & 255;
    const u32 d0 = kin[0], d1 = kin[1], d2 = kin[2], d3 = kin[3];
    const u32 d4 = __builtin_amdgcn_update_dpp(knx[0], d0, 0x130, 0xf, 0xf, false), d5 = __builtin_amdgcn_update_dpp(knx[1], d1, 0x130, 0xf, 0xf, false);
    const u32 e0_ = __builtin_amdgcn_alignbit(d1, d0, 16), e1 = __builtin_amdgcn_alignbit(d2, d1, 16), e2 = __builtin_amdgcn_alignbit(d3, d2, 16),
              e3 = __builtin_amdgcn_alignbit(d4, d3, 16), e4 = __builtin_amdgcn_alignbit(d5, d4, 16);
    LAS char* p = lp(L_F + c * 4 * KC + (t8 + 2) * 16);
    *(LAS u32x4*)(p) = u32x4{d0, d1, d2, d3};
    *(LAS u32x4*)(p + KC) = u32x4{e0_, e1, e2, e3};
    *(LAS u32x4*)(p + 2 * KC) = u32x4{d1, d2, d3, d4};
    *(LAS u32x4*)(p + 3 * KC) = u32x4{e1, e2, e3, e4};
    if (t8 == 0) {
      LAS char* z = lp(L_F + c * 4 * KC);
#pragma unroll
      for (int s = 0; s < 4; ++s) *(LAS u32x4*)(z + s * KC) = u32x4{0u, 0u, 0u, 0u};
      *(LAS u32x4*)(z + 16) = u32x4{0u, 0u, 0u, 0u};
      *(LAS u32x4*)(z + 16 + KC) = u32x4{0u, 0u, 0u, d0 << 16};
      *(LAS u32x4*)(z + 16 + 2 * KC) = u32x4{0u, 0u, 0u, d0};
      *(LAS u32x4*)(z + 16 + 3 * KC) = u32x4{0u, 0u, d0 << 16, e0_};
    }
    if (t8 >= 64 && t8 < 94) *(LAS u32x4*)lp(L_U + c * US + (t8 - 64) * 16) = u32x4{0u, 0u, 0u, 0u};
  }
  TS(4);
  if (w < 7) WAITV((XL == 0 ? 2 : 0) + NAU);
  TSF(20);
  RAWBAR();
  TSF(21);
  LDGU(LDC4);
  TSF(22);
  RAWBAR();
  TSF(23);
#pragma unroll
  for (int e = 0; e < evs(-1); ++e) {
    if (SCH[e].k == K_PH) STC4(ones, ho, rs, 0);
    else if (SCH[e].k == K_XS) { if (SCH[e].a == 0) XST(); }
    else DMAW(SCH[e].a);
  }
  TS(5);
  WAITV(evc(-1, evs(-1), false)); TIEU();
  if (RARE(GBADU())) {
#pragma unroll 1
    for (int it = 0; it < MAXSPIN; ++it) {
      if (GBADU()) LDGU(LDC4P);
      WAITV(0); TIEU();
      if (allz(GBADU())) break;
    }
  }
  u32x4 W[28];
  const u32 wb = (4 * (lane >> 5) + ((lane >> 4) & 1)) * 1024 + ((lane & 15) >> 2) * 256 +
                 ((w ^ (4 * ((lane >> 4) & 1) + ((lane & 15) >> 2))) * 32) + (lane & 3) * 8;
  const LAS char* wbA = lp(opq(wb + L_G0));
  const LAS char* wbB = lp(opq(wb + L_G0 + 49152));
#define LDW(i) W[i] = wslot(i) >= L_G0 + 49152 ? trd2(wbB + (wslot(i) - L_G0 - 49152), 2048) : trd2(wbA + (wslot(i) - L_G0), 2048)
  const u32 sel1 = (lane & 15) == ((lane >> 4) & 1) ? 0x3f803f80u : 0u;
  const u32x4 sel = {sel1, sel1, sel1, sel1};
  f32x4 cs[2] = {{0.f, 0.f, 0.f, 0.f}, {0.f, 0.f, 0.f, 0.f}};
#define CSUM(i) cs[(i) & 1] = mfma16(W[i], sel, cs[(i) & 1])
  float xp[8], gg[8];
  u32x4 xs[7], fh;
  u32 xso[7];
#pragma unroll
  for (int i = 0; i < 7; ++i) { const int s = w + 8 * i, tl = s >= 28, kk = s - 28 * tl + rot; xso[i] = tl * 28672 + (kk >= 28 ? kk - 28 : kk) * 1024; }
  const u32 xo = OFF_H + 2 * P * 28672 + lane * 16, xo2 = xo + 32 * 28672;
#define LDX(i, vo) LDC4(xs[i], vo, rs, xso[i], 0)
#define DMAX2(i) DMAC(L_XB + xco(8 * (i)) + w * 1024, xo2, rs, xso[i])
#define DMAX(i) DMAC((i) * 8192 + w * 1024, xo, rs, xso[i])
  const u32 fho = OFF_FP + ((P >> 2) + (w & 2) * 2) * 2048 + (w & 1) * 896 + (lane < 56 ? lane : 55) * 16;
#define FHBAD() (fh[0] != t0 || fh[1] != t1 || fh[2] != t0 || fh[3] != t1)
  const LAS char* pr = lp(L_PE + (w & 3) * 512 + (2 * (lane >> 3) + (tq >> 2)) * 32 + (((tq & 3) ^ ((lane >> 4) & 2)) << 3));
#define CONV_EPI() do { \
    float y[8]; \
    _Pragma("unroll") for (int i = 0; i < 8; ++i) y[i] = 0.f; \
    _Pragma("unroll") for (int c = 0; c < 2; ++c) \
    _Pragma("unroll") for (int i = 0; i < 4; ++i) { \
      const u32x2 p_ = *(const LAS u32x2*)(pr + (c * 4 + i) * 2048); const u32 p0 = p_[0], p1 = p_[1]; \
      y[4 * c] += bf_lo(p0); y[4 * c + 1] += bf_hi(p0); y[4 * c + 2] += bf_lo(p1); y[4 * c + 3] += bf_hi(p1); } \
    const u32 a0 = pk2(xp[0] + gg[0] * y[0], xp[1] + gg[1] * y[1]), a1 = pk2(xp[2] + gg[2] * y[2], xp[3] + gg[3] * y[3]); \
    const u32 b0 = pk2(xp[4] + gg[4] * y[4], xp[5] + gg[5] * y[5]), b1 = pk2(xp[6] + gg[6] * y[6], xp[7] + gg[7] * y[7]); \
    const bool od = tq & 1; \
    const u32 r0 = __builtin_amdgcn_mov_dpp(od ? a0 : b0, 0xb1, 0xf, 0xf, true), r1 = __builtin_amdgcn_mov_dpp(od ? a1 : b1, 0xb1, 0xf, 0xf, true); \
    const u32x4 o = {nop32(od ? r0 : a0), nop32(od ? r1 : a1), nop32(od ? b0 : r0), nop32(od ? b1 : r1)}; \
    STC4(o, ho, rs, 0); } while (0)
  u32x4 st0, st1;
  u32x2 fa;
  u32 o4[4];
  const u32 s2o = OFF_S2 + (2 * P * 32 + (lane & 31)) * 16;
  const int hf = xcd & 1, g7 = bi >> 2;
  const int t2 = w >> 1, kh = w & 1;
  const u32 dvo = (lane >> 3) * (D * 2) + (((lane & 7) ^ ((lane >> 2) & 4)) << 4) + g7 * 128;
  const u32 dso = (896 * hf + 8 * w) * (D * 2);
  u32 qso[7];
#pragma unroll
  for (int j = 0; j < 7; ++j) { const int g = j + (RQ ? g7 + 1 : 0); qso[j] = (g >= 7 ? g - 7 : g) * 8192; }
#define DMA2(p) DMA((p) * 8192 + w * 1024, dvo, r2, dso + qso[(p) % 7] * 14 + ((p) / 7) * 64 * (D * 2))
  const int l28 = lane < 28 ? lane : 27;
  const u32 fzo = OFF_FA + 16 * 112 * 8 + (P * 16 + (lane & 15)) * 8;
  const u32 fao = OFF_FA + (P * 112 + (lane < 28 ? hf * 56 + 4 * kh + 8 * (lane >> 2) + (lane & 3) : (hf ^ 1) * 56 + 8 * g7 + w)) * 8;
#define AST(t) do { const u32x4 o_ = {nop32(o4[0]), nop32(o4[1]), nop32(o4[2]), nop32(o4[3])}; const u32 a_ = ao + (t) * 112 * 1024; STC4(o_, a_, rs, 0); } while (0)
#define S2OFF(t) ((t) == 0 ? 0 : (t) == 1 ? 512 : (t) == 2 ? 16384 : 16896)
#define XPREP(n) do { \
    static_assert((n) >= 0); WAITV(n); TIEX(); \
    if (RARE(GBADX())) { \
      _Pragma("unroll 1") for (int it = 0; it < MAXSPIN; ++it) { \
        RCNT(1); \
        if (GBADX()) LDGX(LDC4P); \
        WAITV(0); TIEX(); \
        if (allz(GBADX())) break; } \
    } \
    _Pragma("unroll") for (int j = 0; j < 4; ++j) { \
      const u32 sw = j == 0 ? gs0[0] : j == 1 ? gs0[2] : j == 2 ? gs1[0] : gs1[2]; \
      const float mean = bf_lo(sw), rsd = __builtin_amdgcn_rcpf(bf_hi(sw)); \
      const u32 x0 = gx0[j & 2], x1 = gx1[j & 2], v0 = gu0[2 - (j & 2)], v1 = gu1[2 - (j & 2)]; \
      const float xa = (j & 1) ? bf_hi(x0) : bf_lo(x0), xb_ = (j & 1) ? bf_hi(x1) : bf_lo(x1); \
      const float ua = (j & 1) ? bf_lo(v0) : bf_hi(v0), ub = (j & 1) ? bf_lo(v1) : bf_hi(v1); \
      gg[j] = (xa - mean) * rsd; gg[4 + j] = (xb_ - mean) * rsd; \
      xp[j] = xa + gg[j] * (ua * bf_lo(dd)); xp[4 + j] = xb_ + gg[4 + j] * (ub * bf_hi(dd)); \
    } } while (0)
  auto emit = [&](auto ic) __attribute__((always_inline)) {
    constexpr int e0 = evs(decltype(ic)::v), ne = evs(decltype(ic)::v + 1) - e0;
    SFOR(j, ne)
      constexpr int e = e0 + j, kd = SCH[e].k, a = SCH[e].a;
      if constexpr (kd == K_W) DMAW(a);
      if constexpr (kd == K_GL && a == 0) { if (w < 4) LDGX(LDC4); }
      if constexpr (kd == K_GH && a == 0) { if (w >= 4) LDGX(LDC4); }
      if constexpr (kd == K_WH) { if (w >= 4) { DMAW(a); DMAW4(a); } }
      if constexpr (kd == K_PH) STC4(ones, ho, rs, 0);
      if constexpr (kd == K_FH) {
        if constexpr (PHP >= 2) WAIT2(evc(evi(K_PH, 0), e, false), evc(evi(K_PH, 0), e, true));
        const u32x2 f = {t0, t1}; const u32 fo = OFF_FP + (w * 256 + jc) * 8; STF(f, fo, rs); }
      if constexpr (kd == K_PZ) PZ(a);
      if constexpr (kd == K_FL) { if (w >= 4) LDC4(fh, fho, rs, 0, 0); }
      if constexpr (kd == K_FA) { if constexpr (!PZE) WAIT2(evc(evi(K_PZ, SPZ ? 4 : 3), e, false), evc(evi(K_PZ, SPZ ? 4 : 3), e, true));
        const u32x2 f = {t0, t1}; const u32 fo = OFF_FA + (P * 112 + G * 8 + w) * 8; STF(f, fo, rs); }
      if constexpr (kd == K_HS) { if (w < 4) CONV_EPI(); }
      if constexpr (kd == K_HS2) { if (w >= 4) CONV_EPI(); }
      if constexpr (kd == K_PQ && !SPZ && !PZE) { if (w < 4) PZ(4); }
      if constexpr (kd == K_PQ2 && !SPZ && !PZE) { if (w >= 4) PZ(4); }
      if constexpr (kd == K_X) { if constexpr (XD == 2) DMAX(a); else LDX(a, xo); }
      if constexpr (kd == K_X2) { if constexpr (XD) DMAX2(a); else LDX(a, xo2); }
      if constexpr (kd == K_D2) DMA2(a);
      if constexpr (kd == K_S2) { if constexpr (a & 1) LDC4(st1, s2o, rs, S2OFF(a), 0); else LDC4(st0, s2o, rs, S2OFF(a), 0); }
      if constexpr (kd == K_FAL) { if constexpr (SPZ && a == 0) LDC2(fa, fzo, rs, 0); else if constexpr (a == 2) LDC2(fa, fzo, rs, 2048); else LDC2(fa, fao, rs, 0); }
      if constexpr (kd == K_AS) AST(a);
    SEND
  };
  TS(6);
  emit(IC<-1>{});
  {
    LAS char* up = lp(L_U + (30 + etb * 4 + (tq >> 1)) * 16 + ((tq & 1) ^ 1) * 8);
    *(LAS u32x2*)up = u32x2{gu0[0], gu0[2]};
    *(LAS u32x2*)(up + US) = u32x2{gu1[0], gu1[2]};
  }
  bar();
  TS(7);
  {
    const int cc = w >> 2, p = (w >> 1) & 1, qq = w & 1, r = lane & 15, g = lane >> 4;
    const int s = (r + 1) & 3;
    const LAS char* ab = lp(L_F + (cc * 4 + s) * KC + 2 * (32 * p + 25 + r - 8 * g - s) + qq * 128);
    const LAS char* bb = lp(L_U + cc * US + (4 - 4 * p + 2 * r + g) * 16 + (1 - qq) * 128);
    LAS char* pb = lp(L_PE + w * 2048 + 32 * r + 8 * (g ^ ((r >> 2) & 2)));
    u32x4 A[24], B[16];
    f32x4 acc[8];
#define LDA(i, m) do { const u32x2 lo_ = *(const volatile LAS u32x2*)(ab + (m) * 256), hi_ = *(const volatile LAS u32x2*)(ab + (m) * 256 + 8); \
                       A[i] = u32x4{lo_[0], lo_[1], hi_[0], hi_[1]}; } while (0)
#define LDBU(j) B[j] = *(const LAS u32x4*)(bb + (j) * 256)
#define PUT(t) *(LAS u32x2*)(pb + 512 * ((t) & 3)) = u32x2{pk2(acc[t][0], acc[t][1]), pk2(acc[t][2], acc[t][3])}
    LDA(0, 0); LDBU(1); LDBU(3); LDBU(5); LDBU(7); LDA(1, 1); LDBU(0); LDBU(2); LDBU(4); LDBU(6); LDA(2, 2);
#pragma unroll
    for (int t = 0; t < 8; ++t) acc[t] = f32x4{0.f, 0.f, 0.f, 0.f};
    SB();
    SFOR(i, 24)
      constexpr int m = i < 8 ? i : i - 8;
      TSC(4, i);
      if constexpr (i == 9) { TSF(27); WAITB(i); TSF(28); bar(); TS(8); }
      if constexpr (i == 14) { WAITB(i); bar(); TS(10); }
      if constexpr (i == 19) {
        if (w < 4) WAITV(wcnt(i, evs(i), false));
        else {
          WAITV(imin(wcnt(i, evs(i), true), evc(evi(K_FL, 0), evs(i), true))); TIE(fh);
          if (RARE(FHBAD())) {
#pragma unroll 1
            for (int it = 0; it < MAXSPIN; ++it) {
              LDC4(fh, fho, rs, 0, 0); WAITV(0); TIE(fh);
              if (allz(FHBAD())) break;
            }
          }
        }
        bar(); TS(11);
      }
      if constexpr (i + 3 < 24) LDA(i + 3, i + 3 < 8 ? i + 3 : i + 3 - 8);
      SFOR(j, 28) if constexpr (LWS[j] == i) LDW(j); SEND
      if constexpr (i >= 2 && i < 6) { LDBU((i < 4 ? 9 : 8) + 4 * (i & 1)); LDBU((i < 4 ? 11 : 10) + 4 * (i & 1)); }
      if constexpr (i < 8) {
#pragma unroll
        for (int t = m / 2; t < 4; ++t) acc[t] = mfma16(A[i], B[2 * t - m + 1], acc[t]);
      } else {
#pragma unroll
        for (int t = (m / 2 > 4 ? m / 2 : 4); t < 8; ++t) acc[t] = mfma16(A[i], B[2 * t - m + 1], acc[t]);
      }
      SFOR(j, 28) if constexpr (CSS[j] == i) CSUM(j); SEND
      if constexpr (i == 2 || i == 4 || i == 6 || i == 8) PUT(i / 2 - 1);
      if constexpr (i == 18 || i == 20 || i == 22) PUT(i / 2 - 5);
      if constexpr (i == XCK) { TSF(24); if (w < 4) XPREP(evc(evi(K_GL, 3), evs(i), false)); TSF(25); }
      if constexpr (i == XPH) { if (w >= 4) XPREP(evc(evi(K_GH, 3), evs(i), true)); }
      emit(IC<i>{});
      if constexpr (i == 9) TS(9);
      SB();
    SEND
    PUT(7);
  }
  emit(IC<24>{});
  WAITB(25);
  bar();
  TS(12);
  SFOR(j, 28) if constexpr (LWS[j] == 25) LDW(j); SEND
  SFOR(j, 28) if constexpr (CSS[j] == 25) CSUM(j); SEND
  emit(IC<25>{});
  if constexpr (C1) {
    SFOR(j, 28) if constexpr (CSS[j] == 26) CSUM(j); SEND
    TSF(15);
    WAITB(26);
    bar();
    SFOR(j, 28) if constexpr (LWS[j] == 26) LDW(j); SEND
    emit(IC<26>{});
    SFOR(j, 28) if constexpr (CSS[j] == 27) CSUM(j); SEND
  } else {
    SFOR(j, 28) if constexpr (CSS[j] == 26) CSUM(j); SEND
    TSF(15);
  }
  WAIT2(imin(wcnt(100, evs(27), false), evc(evi(K_X, 6), evs(27), false)), imin(wcnt(100, evs(27), true), evc(evi(K_X, 6), evs(27), true)));
#define XCHECK(vo) do { u32 m_ = 0; \
    _Pragma("unroll") for (int i_ = 0; i_ < 7; ++i_) { TIE(xs[i_]); m_ = umax3(m_, xs[i_][0], xs[i_][1]); m_ = umax3(m_, xs[i_][2], xs[i_][3]); } \
    if (RARE(m_ == ~0u)) {  \
      _Pragma("unroll 1") for (int it = 0; it < MAXSPIN; ++it) { \
        RCNT(0); \
        _Pragma("unroll") for (int i_ = 0; i_ < 7; ++i_) LDX(i_, vo); \
        WAITV(0); m_ = 0; \
        _Pragma("unroll") for (int i_ = 0; i_ < 7; ++i_) { TIE(xs[i_]); m_ = umax3(m_, xs[i_][0], xs[i_][1]); m_ = umax3(m_, xs[i_][2], xs[i_][3]); } \
        if (allz(m_ == ~0u)) break; } \
       } } while (0)
  TS(13);
  LAS char* xw = lp(w * 1024 + lane * 16);
  LAS char* xwB = lp(opq(L_XB + w * 1024 + lane * 16));
#define XM4(m, i) umax3(umax3(m, xs[i][0], xs[i][1]), xs[i][2], xs[i][3])
#define XM7() XM4(XM4(XM4(XM4(XM4(XM4(XM4(0u, 0), 1), 2), 3), 4), 5), 6)
  u32 xm;
  if constexpr (XD == 2) {
#pragma unroll
    for (int i = 0; i < 7; ++i) xs[i] = *(const volatile LAS u32x4*)(xw + i * 8192);
    xm = XM7();
    if (RARE(xm == ~0u)) {
#pragma unroll 1
      for (int it = 0; it < MAXSPIN; ++it) {
        RCNT(0);
        DMAX(0); DMAX(1); DMAX(2); DMAX(3); DMAX(4); DMAX(5); DMAX(6);
        WAITV(0);
#pragma unroll
        for (int i = 0; i < 7; ++i) xs[i] = *(const volatile LAS u32x4*)(xw + i * 8192);
        xm = XM7();
        if (allz(xm == ~0u)) break;
      }
    }
  } else {
    XCHECK(xo);
#pragma unroll
    for (int i = 0; i < 5; ++i) *(LAS u32x4*)(xw + i * 8192) = xs[i];
  }
  bar();
  TS(14);

  const u32 xb_ = (8 * (lane >> 5) + ((lane & 15) >> 2)) * 64 + ((lane >> 4) & 1) * 32 + (lane & 3) * 8;
  const LAS char* xbA = lp(opq(xb_));
  const LAS char* xbB = lp(opq(xb_ + L_XB));
  const u32 bo = OFF_A + ((P * 4 + t2) * 112 + hf * 56 + 4 * kh) * 1024 + lane * 16;
  u32 rso;
  { const int kk = 4 * g7 + 2 * hf + kh - rot, s = (t2 & 1) * 28 + (kk < 0 ? kk + 28 : kk);
    rso = xb_ - (lane >> 5) * 256 + (t2 < 2 ? s * 1024 : L_XB + xco(s)); }
#define LDQ(n) asm volatile("buffer_load_dwordx4 %0, %1, %2, %3 offen offset:%4 sc1" : "+v"(Bq[n]) : "v"(bo), "s"(rs), "s"(qso[(n) >> 2]), "i"(((n) & 3) * 1024) : "memory")
#define QSO(n) qso[(n) >> 2]
#define QIO(n) (((n) & 3) * 1024)
#define QL(d, so, io) "buffer_load_dwordx4 %" #d ", %[bo], %[rs], %[" #so "] offen offset:%[" #io "] sc1\n\t"
#define QIN [bo] "v"(bo), [rs] "s"(rs), [t2] "s"(t2)
#define LDQ03(n) asm volatile("s_cmp_eq_u32 %[t2], 0\n\ts_cbranch_scc0 .Lq%=\n\t" QL(0, s0, i0) QL(1, s1, i1) QL(2, s2, i2) ".Lq%=:" \
    : "=v"(Bq[n]), "=v"(Bq[(n) + 1]), "=v"(Bq[(n) + 2]) : QIN, [s0] "s"(QSO(n)), [i0] "i"(QIO(n)), [s1] "s"(QSO((n) + 1)), [i1] "i"(QIO((n) + 1)), \
      [s2] "s"(QSO((n) + 2)), [i2] "i"(QIO((n) + 2)) : "memory", "scc")
#define LDQ0W() asm volatile("s_cmp_eq_u32 %[t2], 0\n\ts_cbranch_scc1 .Lr%=\n\ts_waitcnt vmcnt(0)\n\ts_branch .Lq%=\n.Lr%=:\n\ts_waitcnt vmcnt(12)\n\t" \
    QL(0, s0, i0) QL(1, s0, i1) ".Lq%=:" : "=v"(Bq[12]), "=v"(Bq[13]) : QIN, [s0] "s"(QSO(12)), [i0] "i"(0), [i1] "i"(1024) : "memory", "scc")
#define LDQ01(n, m) asm volatile("s_cmp_eq_u32 %[t2], 1\n\ts_cbranch_scc1 .Lr%=\n\ts_cmp_eq_u32 %[t2], 0\n\ts_cbranch_scc0 .Lq%=\n\t" \
    QL(0, s0, i0) QL(1, s0, i1) "s_branch .Lq%=\n.Lr%=:\n\t" QL(2, s1, j0) QL(3, s1, j1) QL(4, s1, j2) QL(5, s1, j3) ".Lq%=:" \
    : "=v"(Bq[n]), "=v"(Bq[(n) + 1]), "+v"(Bq[m]), "+v"(Bq[(m) + 1]), "+v"(Bq[(m) + 2]), "+v"(Bq[(m) + 3]) \
    : QIN, [s0] "s"(QSO(n)), [i0] "i"(QIO(n)), [i1] "i"(QIO((n) + 1)), [s1] "s"(QSO(m)), [j0] "i"(0), [j1] "i"(1024), [j2] "i"(2048), [j3] "i"(3072) : "memory", "scc")
#define LDQ01L() asm volatile("s_cmp_eq_u32 %[t2], 0\n\ts_cbranch_scc1 .Lr%=\n\ts_cmp_eq_u32 %[t2], 1\n\ts_cbranch_scc0 .Lq%=\n\t" \
    QL(0, s1, j0) QL(1, s1, j1) ".Lr%=:\n\t" QL(2, s1, j2) QL(3, s1, j3) ".Lq%=:" \
    : "+v"(Bq[24]), "+v"(Bq[25]), "=v"(Bq[26]), "=v"(Bq[27]) \
    : QIN, [s1] "s"(QSO(24)), [j0] "i"(0), [j1] "i"(1024), [j2] "i"(2048), [j3] "i"(3072) : "memory", "scc")
#define QL4(a, b, c, d, so) "buffer_load_dwordx4 %" #a ", %[bo], %[rs], %[" #so "] offen sc1\n\tbuffer_load_dwordx4 %" #b ", %[bo], %[rs], %[" #so "] offen offset:1024 sc1\n\t" \
    "buffer_load_dwordx4 %" #c ", %[bo], %[rs], %[" #so "] offen offset:2048 sc1\n\tbuffer_load_dwordx4 %" #d ", %[bo], %[rs], %[" #so "] offen offset:3072 sc1\n\t"
#define BQ4(n) "+v"(Bq[n]), "+v"(Bq[(n) + 1]), "+v"(Bq[(n) + 2]), "+v"(Bq[(n) + 3])
#define BQ4D(n) "=v"(Bq[n]), "=v"(Bq[(n) + 1]), "=v"(Bq[(n) + 2]), "=v"(Bq[(n) + 3])
#define LDQ04(n) asm volatile("s_cmp_eq_u32 %[t2], 0\n\ts_cbranch_scc0 .Lq%=\n\t" QL4(0, 1, 2, 3, s0) "\n.Lq%=:" : BQ4D(n) : QIN, [s0] "s"(QSO(n)) : "memory", "scc")
#define LDQ14(n) asm volatile("s_cmp_eq_u32 %[t2], 1\n\ts_cbranch_scc0 .Lq%=\n\t" QL4(0, 1, 2, 3, s0) "\n.Lq%=:" : BQ4(n) : QIN, [s0] "s"(QSO(n)) : "memory", "scc")
#define LDQW(c) asm volatile("s_cmp_eq_u32 %0, 0\n\ts_cbranch_scc1 .Lr%=\n\ts_waitcnt vmcnt(0)\n.Lr%=:\n\ts_waitcnt vmcnt(%1)" :: "s"(t2), "i"(c) : "memory", "scc")
#define LDQALL(c) do { \
    asm volatile("s_cmp_eq_u32 %[t2], " #c "\n\ts_cbranch_scc0 .Lq%=\n\t" QL4(0, 1, 2, 3, s0) QL4(4, 5, 6, 7, s1) QL4(8, 9, 10, 11, s2) QL4(12, 13, 14, 15, s3) ".Lq%=:" \
      : BQ4(0), BQ4(4), BQ4(8), BQ4(12) : QIN, [s0] "s"(qso[0]), [s1] "s"(qso[1]), [s2] "s"(qso[2]), [s3] "s"(qso[3]) : "memory", "scc"); \
    asm volatile("s_cmp_eq_u32 %[t2], " #c "\n\ts_cbranch_scc0 .Lq%=\n\t" QL4(0, 1, 2, 3, s0) QL4(4, 5, 6, 7, s1) QL4(8, 9, 10, 11, s2) \
      "buffer_load_dword %[jk], %[bo], %[rs], 0 offen sc1\n.Lq%=:" \
      : BQ4(16), BQ4(20), BQ4(24), [jk] "+v"(junk) : QIN, [s0] "s"(qso[4]), [s1] "s"(qso[5]), [s2] "s"(qso[6]) : "memory", "scc"); } while (0)
  u32 junk = 0;
  u32x4 Bf[4][28], Bq[28], res, c4a, c4b;
  f32x16 acc[2];
  float nm, karg, cv, hv0, ga, up, ee;
#define XSL(t, ks) (((t) & 1) * 28 + (ks))
#define LDB(t, ks) Bf[t][ks] = (t) < 2 ? trd2(xbA + XSL(t, ks) * 1024, 256) : trd2(xbB + xco(XSL(t, ks)), 256)
#define XW2(i) *(LAS u32x4*)(xwB + xco(8 * (i))) = xs[i]
#define XR2(i) xs[i] = *(const volatile LAS u32x4*)(xwB + xco(8 * (i)))
#define S2CHECK(st, t) do { TIE(st); \
    if (RARE(gbad(st, tA, tB))) {  \
      _Pragma("unroll 1") for (int it = 0; it < MAXSPIN; ++it) { \
        LDC4(st, s2o, rs, S2OFF(t), 0); WAITV(0); TIE(st); \
        if (allz(gbad(st, tA, tB))) break; } \
       } \
    { const u32 a_ = st[0], b_ = st[2]; nm = __uint_as_float(a_); cv = __uint_as_float(b_); karg = -1.44269504088896340736f * __builtin_amdgcn_rsqf(cv); } } while (0)
#define CSLD(h) do { c4a = *(const LAS u32x4*)lp(L_CS + w * 128 + (lane >> 5) * 16 + (h) * 32); \
                     c4b = *(const LAS u32x4*)lp(L_CS + w * 128 + (lane >> 5) * 16 + (h) * 32 + 64); } while (0)
#define EPI_A(a, i) do { const u32 ca_ = c4a[(i) & 3], cb_ = c4b[(i) & 3]; TIE(nm); ga = a[i] + nm * __uint_as_float(ca_); TIE(ga); up = a[(i) + 8] + nm * __uint_as_float(cb_); \
                         ee = __builtin_amdgcn_exp2f(ga * karg); TIE(ee); TIE(up); } while (0)
#define EPI_B(i) do { TIE(ee); float h_ = (ga * up) * __builtin_amdgcn_rcpf(ee * cv + cv); TIE(h_); if ((i) & 1) { o4[(i) >> 1] = pk2(hv0, h_); TIE(o4[(i) >> 1]); } else hv0 = h_; } while (0)
  SFOR(j, PD1) LDB(0, j); SEND
  SB();
  SFOR(t, 4)
#pragma unroll
    for (int i = 0; i < 16; ++i) acc[t & 1][i] = 0.f;
    SFOR(ks, 28)
      constexpr int e0s = t == 1 ? E1 : 2;
      constexpr int gs = 100 + 28 * t + ks;
      TSC(t, ks);
      if constexpr (t == 0 && ks == B0 && LW1) WAITB(gs);
      if constexpr (t == 0 && ks == B0) bar();
      if constexpr ((t == 1 && !NB12 && ks == 2) || (t == 2 && ks == B2)) RAWBAR();
      if constexpr (t == 1 && ks == B1) bar();
      if constexpr (PRI > 0) {
        constexpr int pa = prio_(gs, PRO), pb = prio_(gs, -1);
        if constexpr (gs == 100 || pa != prio_(gs - (gs > 100), PRO) || pb != prio_(gs - (gs > 100), -1)) {
          if constexpr (pa == pb) __builtin_amdgcn_s_setprio(pa);
          else { if (w < 4) __builtin_amdgcn_s_setprio(pa); else __builtin_amdgcn_s_setprio(pb); } } }
      if constexpr (ks + PD1 < 28) LDB(t, ks + PD1);
      if constexpr (ks + PD1 >= 28 && t < 3) LDB(t + 1, ks + PD1 - 28);
      if constexpr (t == 0) { SFOR(j, 28) if constexpr (LWS[j] == gs) LDW(j); SEND }
      acc[t & 1] = mfma32(W[ks], Bf[t][ks], acc[t & 1]);
      if constexpr (ks == 4) if (t == t2) res = trd2(lp(rso), 512);
      if constexpr (t == 0) {
        if constexpr (XD < 2 && ks < 2) *(LAS u32x4*)(xw + (ks + 5) * 8192) = xs[ks + 5];
        SFOR(j, 28) if constexpr (CSS[j] == gs) CSUM(j); SEND
        if constexpr (ks == 27) { const f32x4 c = cs[0] + cs[1]; if ((lane & 15) < 2) *(LAS f32x4*)lp(L_CS + w * 128 + (lane & 1) * 64 + (lane >> 4) * 16) = c; }
      } else {
        if constexpr (ks == e0s - 2) CSLD(0);
        if constexpr (ks == 27 && t < 3) CSLD(0);
        if constexpr (ks == e0s - 1) { WAITE(K_S2, t == 3 ? 3 : t - 1, gs); if constexpr (t == 2) S2CHECK(st1, 1); else S2CHECK(st0, t - 1); }
        if constexpr (ks >= e0s && ks < e0s + 16) { if constexpr ((ks - e0s) & 1) EPI_B((ks - e0s) >> 1); else EPI_A(acc[(t - 1) & 1], (ks - e0s) >> 1); }
        if constexpr (ks == e0s + 6) CSLD(1);
        if constexpr (t == 3 && ks == e0s + 16) AST(2);
        static_assert(t == 3 || SCH[evi(K_AS, t < 3 ? t - 1 : 0)].step >= 100 + 28 * t + e0s + 16);
        if constexpr (t == 1) {
          if constexpr (!XD && ks == XC) { WAITE(K_X2, 6, gs); XCHECK(xo2); }
          if constexpr (!XD && ks > XC && ks < XC + 8) XW2(ks - XC - 1);
          if constexpr (XD && ks == XC) { WAITE(K_X2, 6, gs); XR2(0); XR2(1); XR2(2); XR2(3); }
          if constexpr (XD && ks == XC + 1) { XR2(4); XR2(5); XR2(6); }
          if constexpr (XD && ks == XC + 2) xm = XM4(XM4(XM4(XM4(0u, 0), 1), 2), 3);
          if constexpr (XD && ks == XC + 3) {
            xm = XM4(XM4(XM4(xm, 4), 5), 6);
            if (RARE(xm == ~0u)) {
#pragma unroll 1
              for (int it = 0; it < MAXSPIN; ++it) {
                RCNT(0);
                DMAX2(0); DMAX2(1); DMAX2(2); DMAX2(3); DMAX2(4); DMAX2(5); DMAX2(6);
                WAITV(0);
                XR2(0); XR2(1); XR2(2); XR2(3); XR2(4); XR2(5); XR2(6);
                xm = XM7();
                if (allz(xm == ~0u)) break;
              }
              __builtin_amdgcn_s_waitcnt(0xc07f);
            }
          }
          if constexpr (SPZ && ks == FCK) { WAITE(K_FAL, 0, gs); TIE(fa); flagwait(fa, fzo, rs, t0, t1); }
        }
        if constexpr (t == 2) {
          if constexpr (SP2 && ks == 16) { WAITE(K_FAL, 2, gs); TIE(fa); flagwait(fa, fzo + 2048, rs, t0, t1); }
          if constexpr (ks == 20) *(LAS u32x2*)lp(L_EF + w * 8) = u32x2{0u, 0u};
        }
        if constexpr (t == 3) {
          if constexpr (!SPZ && ks == FCK) { WAITV(evc(evi(K_FAL, 0), NEV, false) + (FCK == 4)); TIE(fa); flagwait(fa, fao, rs, t0, t1); }
          if constexpr (SPZ == 2 && ks == 2) { WAITV(evc(evi(K_FAL, 1), NEV, false)); TIE(fa); }
          if constexpr (!XCONT && ks == 2) RAWBAR();
          if constexpr (!XCONT && (ks == 3 || ks == 4)) DMA2(9 + ks);
          if constexpr (!QE) {
            if constexpr (ks >= 5 && ks < 13 && (ks & 1)) LDQ03(3 * ((ks - 5) / 2));
            if constexpr (ks == 13) LDQ0W();
            if constexpr (ks >= 15 && ks < 27 && (ks & 1)) LDQ01(ks - 1, 2 * (ks - 15));
            if constexpr (ks == 27) LDQ01L();
          } else {
            if constexpr (ks == 13) LDQW(2 * (13 - (QE > 5 ? QE : 5)));
            if constexpr (ks >= QE && ks < QE + 14 && ((ks - QE) & 1) == 0) LDQ04(2 * (ks - QE));
            if constexpr (ks >= 15 && (ks & 1)) LDQ14(2 * (ks - 15));
          }
          if constexpr (ks == 27) { if constexpr (!PLS) RAWBAR(); }
        }
      }
      emit(IC<gs>{});
      SB();
    SEND
    if constexpr (t == 0) TSG(15); else TS(15 + t);
  SEND
  CSLD(0);
  S2CHECK(st1, 3);
#pragma unroll
  for (int i = 0; i < 8; ++i) { if (i == 4) CSLD(1); EPI_A(acc[1], i); EPI_B(i); }
  AST(3);
  if constexpr (SPZ == 2) flagwait(fa, fao, rs, t0, t1);
  TS(19);

  {
    LDQALL(2);
    if constexpr (PLS) RAWBAR();
    if (w >= 4) __builtin_amdgcn_s_setprio(1); else if (PRI > 0) __builtin_amdgcn_s_setprio(0);
    u32x4 Af[56];
    const u32 vb_ = kh * 57344 + (4 * (lane >> 5) + ((lane & 15) >> 2)) * 128 + ((hf ^ ((lane >> 3) & 1)) * 64) + ((((lane >> 4) ^ kh) & 1) * 32) + (lane & 3) * 8;
    const LAS char* vb = lp(vb_);
    const LAS char* vc = lp(vb_ ^ 64u);
#define LDV(n) Af[n] = (n) < 28 ? trd2(vc + (n) * 2048, 1024) : trd2(vb + ((n) - 28) * 2048, 1024)
    SFOR(j, PD2) LDV(j); SEND
    if constexpr (T3 == 1) { if (w >= 6) { WAITV(0); if constexpr (T3S > 0) __builtin_amdgcn_s_sleep(T3S); TSG(20); } }
    else if (w >= 6) {
      const u32 po = bo - lane * 16 + (l28 >> 2) * 8192 + (l28 & 3) * 1024 + 1020;
      u32 pv;
#pragma unroll 1
      for (int it = 0; it < MAXSPIN; ++it) {
        LDC1(pv, po, rs); WAITV(0); TIE(pv);
        if (allz(pv == ~0u)) break;
      }
      TSG(20);
    }
    LDQALL(3);
    TSG(21);
#pragma unroll
    for (int i = 0; i < 16; ++i) { acc[0][i] = 0.f; acc[1][i] = 0.f; }
    LAS char* ew = lp(L_EX + w * 4096 + lane * 16);
    const LAS char* er = lp(L_EX + (w ^ 1) * 4096 + lane * 16);
    const u32 ro = opq(OFF_Q + ((P * 14 + G) * 8 + w) * 1024 + lane * 16), so0 = opq(QSA);
    f32x4 p0, p1;
    u32x4 g0;
#define EXW(a, ps) do { *(volatile LAS f32x4*)(ew + (ps) * 2048) = f32x4{a[8], a[9], a[10], a[11]}; *(volatile LAS f32x4*)(ew + (ps) * 2048 + 1024) = f32x4{a[12], a[13], a[14], a[15]}; } while (0)
#define EXF(ps) *(volatile LAS u32*)lp(L_EF + w * 8 + (ps) * 4) = 1u
#define EXL(ps) do { ef = *(const volatile LAS u32*)lp(L_EF + (w ^ 1) * 8 + (ps) * 4); \
    p0 = *(const volatile LAS f32x4*)(er + (ps) * 2048); p1 = *(const volatile LAS f32x4*)(er + (ps) * 2048 + 1024); } while (0)
#define EXC(ps) do { if (RARE(ef == 0)) { _Pragma("unroll 1") for (int it = 0; it < MAXSPIN; ++it) { EXL(ps); if (allz(ef == 0)) break; } \
      __builtin_amdgcn_s_waitcnt(0xc07f); } } while (0)
    u32 ef;
#define LDG() LDC4(g0, ro, rs, 0, 0)
#define GBADQ() (umax3(umax3(g0[0], g0[1], g0[2]), g0[3], 0u) == ~0u)
    SB();
    SFOR(n, 56)
      if constexpr ((n & 1) == 0) TSC(5, n / 2);
      if constexpr (n < 28 && (n & 3) == 0) {
        WAITV(25 - n);
        u32 m_ = 0;
#pragma unroll
        for (int i = 0; i < 4; ++i) { TIE(Bq[n + i]); m_ = umax3(m_, Bq[n + i][0], Bq[n + i][1]); m_ = umax3(m_, Bq[n + i][2], Bq[n + i][3]); }
        if (RARE(m_ == ~0u)) {
#pragma unroll 1
          for (int it = 0; it < MAXSPIN; ++it) {
            RCNT(1);
            WAITV(0);
#pragma unroll
            for (int i = n; i < n + 4; ++i) LDQ(i);
            if (it == 0 || !T3) {
#pragma unroll
              for (int i = n + 4; i < 28; ++i) LDQ(i);
              asm volatile("buffer_load_dword %0, %1, %2, 0 offen sc1" : "+v"(junk) : "v"(bo), "s"(rs) : "memory");
              WAITV(25 - n);
            } else WAITV(0);
            m_ = 0;
#pragma unroll
            for (int i = 0; i < 4; ++i) { TIE(Bq[n + i]); m_ = umax3(m_, Bq[n + i][0], Bq[n + i][1]); m_ = umax3(m_, Bq[n + i][2], Bq[n + i][3]); }
            if (allz(m_ == ~0u)) break;
          }
        }
        if constexpr (n == 0) TSG(22);
      }
      if constexpr (n + PD2 < 56) LDV(n + PD2);
      if constexpr (n < 28) acc[1] = mfma32(Af[n], Bq[n], acc[1]); else acc[0] = mfma32(Af[n], Bq[n - 28], acc[0]);
      if constexpr (n == 28) TSG(23);
      if constexpr (n == XW0) { EXW(acc[1], 0); EXF(0); }
      if constexpr (n == XL0) EXL(0);
      if constexpr (n == XL0 + XD0) {
        EXC(0);
        const u32x4 q_ = {nop32(pk2(acc[1][0] + p0[0], acc[1][1] + p0[1])), nop32(pk2(acc[1][2] + p0[2], acc[1][3] + p0[3])),
                          nop32(pk2(acc[1][4] + p1[0], acc[1][5] + p1[1])), nop32(pk2(acc[1][6] + p1[2], acc[1][7] + p1[3]))};
        STC4(q_, so0, rs, 0);
        TSG(24);
      }
      if constexpr (n == GL0) LDG();
      SB();
    SEND
    TSG(25);
    if constexpr (GL0 == 56) LDG();
    EXW(acc[0], 1); EXF(1);
    float y[8];
#pragma unroll
    for (int e = 0; e < 4; ++e) { const u32 r = res[e]; y[2 * e] = acc[0][2 * e] + bf_lo(r); y[2 * e + 1] = acc[0][2 * e + 1] + bf_hi(r); }
#pragma unroll
    for (int e = 0; e < 8; ++e) TIE(y[e]);
#pragma unroll 1
    for (int it = 0; it < MAXSPIN; ++it) { EXL(1); if (allz(ef == 0)) break; }
#pragma unroll
    for (int e = 0; e < 4; ++e) { y[e] += p0[e]; y[4 + e] += p1[e]; }
    TSG(26);
    WAITV(0); TIE(g0); TIE(junk);
    if (RARE(GBADQ())) {
#pragma unroll 1
      for (int it = 0; it < MAXSPIN; ++it) {
        LDG(); WAITV(0); TIE(g0);
        if (allz(GBADQ())) break;
      }
    }
    TSG(27);
    {
#pragma unroll
      for (int e = 0; e < 4; ++e) { const u32 r = g0[e]; y[2 * e] += bf_lo(r); y[2 * e + 1] += bf_hi(r); }
    }
    const u32 x0 = pk2(y[0], y[1]), x1 = pk2(y[2], y[3]), y0 = pk2(y[4], y[5]), y1 = pk2(y[6], y[7]);
    const auto s0 = __builtin_amdgcn_permlane32_swap(x0, y0, false, false), s1 = __builtin_amdgcn_permlane32_swap(x1, y1, false, false);
    const int tok = (t2 < 2 ? 2 * P + t2 : 30 + 2 * P + t2) * 32 + (lane & 31);
#define OST ""
    { const u32x4 o_ = {s0[0], s1[0], s0[1], s1[1]}, ro_ = mksrd(out, SZ_SD);
      const u32 oo = (tok * D + 64 * g7 + 32 * hf + 16 * kh + 8 * (lane >> 5)) * 2;
      asm volatile("buffer_store_dwordx4 %0, %1, %2, 0 offen" OST "\n\ts_nop 1" :: "v"(o_), "v"(oo), "s"(ro_) : "memory"); }
  }
  if (b == 0 && w == 0) { const u32x2 f = {t0, t1}; STF(f, zero, rs); }
  TSOUT();
}

extern "C" void kernel_launch(void* const* d_in, const int* in_sizes, int n_in, void* d_out, int out_size,
                              void* d_ws, size_t ws_size, hipStream_t stream) {
  (void)hipFuncSetAttribute((const void*)fused, hipFuncAttributeMaxDynamicSharedMemorySize, L_SIZE);
  fused<<<256, 512, L_SIZE, stream>>>((const u16*)d_in[0], (const u16*)d_in[1], (const u16*)d_in[2], (const u16*)d_in[3],
                                      (const u16*)d_in[4], (const u16*)d_in[5], (const u16*)d_in[7], (const u16*)d_in[8],
                                      (const u16*)d_in[9], (u16*)d_out, (char*)d_ws + (WST ? ((ws_size - WS_END) & ~(size_t)0xffff) : 0));
}
```
